```python
import torch

def _softmax(x, axis=-1):
    return torch.softmax(x, dim=axis)
def _gelu(x):
    return torch.nn.functional.gelu(x, approximate="tanh")
def _silu(x):
    return torch.nn.functional.silu(x)
def _sigmoid(x):
    return torch.sigmoid(x)
def _layernorm(x, axis=-1, eps=1e-5):
    m = x.mean(dim=axis, keepdim=True)
    v = x.var(dim=axis, keepdim=True, unbiased=False)
    return (x - m) / torch.sqrt(v + eps)
def _scatter_add(tgt, idx, src, axis=0):
    return tgt.clone().index_add_(axis, idx, src)
def _segment_sum(src, seg, n):
    out = torch.zeros((n,) + src.shape[1:], dtype=src.dtype)
    return out.index_add_(0, seg, src)
def _pad(x, pw):
    flat = tuple(v for pair in reversed(pw) for v in pair)
    return torch.nn.functional.pad(x, flat)

kernel_name = 'parallel_sum__mha_block_cached__dense_activation_gelu__decode'

def setup_inputs():

    _w = (lambda a: (lambda t: t.to(torch.bfloat16) if t.is_floating_point() else t)(torch.as_tensor(a)))
    def _A_setup():
        B, Sq, D = 8, 1, 512
        H, Sk, Dh = 32, 4096, 128
        return {
            "x": _w((lambda *s: torch.randn(*s, dtype=torch.bfloat16) * 0.05)(B, Sq, D)),
            "k_cache": _w((lambda *s: torch.randn(*s, dtype=torch.bfloat16) * 3.2)(B, H, Sk, Dh)),
            "v_cache": _w((lambda *s: torch.randn(*s, dtype=torch.bfloat16) * 0.8)(B, H, Sk, Dh)),
            "wq": _w((lambda *s: torch.randn(*s, dtype=torch.bfloat16) * 3.2)(D, H * Dh) * (D ** -0.5)),
            "wo": _w((lambda *s: torch.randn(*s, dtype=torch.bfloat16) * 0.8)(H * Dh, D) * ((H * Dh) ** -0.5)),
        }
    _A_in = _A_setup()

    def _B_setup():
        B, S, D, F = 8, 1, 512, 1792
        return {
            "x": _w((lambda *s: torch.randn(*s, dtype=torch.bfloat16) * 0.05)(B, S, D)),
            "w1": _w((lambda *s: torch.randn(*s, dtype=torch.bfloat16) * 3.2)(D, F) * (D ** -0.5)),
            "b1": _w((lambda *s: torch.randn(*s, dtype=torch.bfloat16) * 3.2)(F) * 0.01),
            "w2": _w((lambda *s: torch.randn(*s, dtype=torch.bfloat16) * 0.4)(F, D) * (F ** -0.5)),
            "b2": _w((lambda *s: torch.randn(*s, dtype=torch.bfloat16) * 0.4)(D) * 0.01),
        }
    _B_in = _B_setup()

    return {
        "A_x": _A_in["x"],
        "A_k_cache": _A_in["k_cache"],
        "A_v_cache": _A_in["v_cache"],
        "A_wq": _A_in["wq"],
        "A_wo": _A_in["wo"],
        "B_x": _B_in["x"],
        "B_w1": _B_in["w1"],
        "B_b1": _B_in["b1"],
        "B_w2": _B_in["w2"],
        "B_b2": _B_in["b2"],
    }

def reference(A_x, A_k_cache, A_v_cache, A_wq, A_wo, B_x, B_w1, B_b1, B_w2, B_b2):
    def _A_ref(x, k_cache, v_cache, wq, wo):
        B, Sq, D = x.shape
        H, Dh = 32, 128
        q = (lambda x, *d: x.permute(*d))((x @ wq).reshape(B, Sq, H, Dh), 0, 2, 1, 3)
        scores = torch.einsum("bhqd,bhkd->bhqk", q, k_cache) * (Dh ** -0.5)
        p = _softmax(scores, axis=-1)
        o = torch.einsum("bhqk,bhkd->bhqd", p, v_cache)
        return (lambda x, *d: x.permute(*d))(o, 0, 2, 1, 3).reshape(B, Sq, H * Dh) @ wo

    def _B_ref(x, w1, b1, w2, b2):
        h = torch.einsum("bsd,df->bsf", x, w1) + b1
        h = _gelu(h)
        return torch.einsum("bsf,fd->bsd", h, w2) + b2

    x = A_x
    xn = _layernorm(x, axis=-1)
    return x + _A_ref(xn, A_k_cache, A_v_cache, A_wq, A_wo) + _B_ref(xn, B_w1, B_b1, B_w2, B_b2)
```

```cpp
#include <hip/hip_runtime.h>
#include <hip/hip_bf16.h>
#include <cstdint>
typedef unsigned short u16;
typedef unsigned u32x4 __attribute__((ext_vector_type(4)));
__device__ __forceinline__ float bf2f(u16 v){ return __builtin_bit_cast(float,(unsigned)v<<16); }
__device__ __forceinline__ float lo(unsigned v){ return __builtin_bit_cast(float,v<<16); }
__device__ __forceinline__ float hi(unsigned v){ return __builtin_bit_cast(float,v&0xffff0000u); }
__device__ __forceinline__ u16 f2bf(float f){ __hip_bfloat16 b = __float2bfloat16(f); return __builtin_bit_cast(u16,b); }
__device__ __forceinline__ float rbf(float f){ return bf2f(f2bf(f)); }
#define MIMIC 0
constexpr int B=8,D=512,H=32,SK=4096,DH=128,F=1792;

__global__ void k_proj(const u16* x,const u16* wq,const u16* w1,const u16* b1,float* q,float* hbuf){
  __shared__ float xn[B][D];
  int tid=threadIdx.x;
  {
    int b=tid>>5, l=tid&31;
    float s=0; for(int i=l;i<D;i+=32) s+=bf2f(x[b*D+i]);
    for(int o=16;o;o>>=1) s+=__shfl_xor(s,o);
    float m=s/D;
    float v=0; for(int i=l;i<D;i+=32){ float d=bf2f(x[b*D+i])-m; v+=d*d; }
    for(int o=16;o;o>>=1) v+=__shfl_xor(v,o);
    v/=D;
    if(MIMIC){
      float mb=rbf(m), vb=rbf(v); float den=rbf(sqrtf(rbf(vb+1e-5f)));
      for(int i=l;i<D;i+=32) xn[b][i]=rbf(rbf(bf2f(x[b*D+i])-mb)/den);
    } else {
      float r=1.f/sqrtf(v+1e-5f);
      for(int i=l;i<D;i+=32) xn[b][i]=(bf2f(x[b*D+i])-m)*r;
    }
  }
  __syncthreads();
  int col=blockIdx.x*256+tid;
  if(col<H*DH){
    float acc[B]={0};
    for(int k=0;k<D;k++){ float w=bf2f(wq[k*(H*DH)+col]); for(int b=0;b<B;b++) acc[b]+=xn[b][k]*w; }
    for(int b=0;b<B;b++) q[b*H*DH+col]=MIMIC?rbf(acc[b]):acc[b];
  } else if(col<H*DH+F){
    int c=col-H*DH;
    float acc[B]={0};
    for(int k=0;k<D;k++){ float w=bf2f(w1[k*F+c]); for(int b=0;b<B;b++) acc[b]+=xn[b][k]*w; }
    for(int b=0;b<B;b++){
      float h=MIMIC? rbf(rbf(acc[b])+bf2f(b1[c])) : acc[b]+bf2f(b1[c]);
      float g=0.5f*h*(1.f+tanhf(0.7978845608028654f*(h+0.044715f*h*h*h)));
      hbuf[b*F+c]=MIMIC?rbf(g):g;
    }
  }
}
__global__ void k_attn(const float* q,const u16* K,const u16* V,float* o){
  __shared__ float sc[SK];
  __shared__ float red[256];
  __shared__ float oacc[16][DH];
  int bh=blockIdx.x, tid=threadIdx.x; int b=bh/H,h=bh%H;
  int c=tid&15, r=tid>>4;
  float qv[8]; for(int j=0;j<8;j++) qv[j]=q[b*H*DH+h*DH+8*c+j];
  const u32x4* Kp=(const u32x4*)(K+(size_t)bh*SK*DH);
  for(int k=r;k<SK;k+=16){
    u32x4 v=Kp[k*16+c];
    float s=0; for(int j=0;j<4;j++) s+=lo(v[j])*qv[2*j]+hi(v[j])*qv[2*j+1];
    for(int o2=8;o2;o2>>=1) s+=__shfl_xor(s,o2);
    if(c==0) sc[k]=MIMIC? rbf(rbf(s)*0.08838834764831845f) : s*0.08838834764831845f;
  }
  __syncthreads();
  float m=-INFINITY; for(int k=tid;k<SK;k+=256) m=fmaxf(m,sc[k]);
  red[tid]=m; __syncthreads();
  for(int s=128;s;s>>=1){ if(tid<s) red[tid]=fmaxf(red[tid],red[tid+s]); __syncthreads(); }
  m=red[0]; __syncthreads();
  float l=0; for(int k=tid;k<SK;k+=256){ float p=__expf(sc[k]-m); sc[k]=p; l+=p; }
  red[tid]=l; __syncthreads();
  for(int s=128;s;s>>=1){ if(tid<s) red[tid]+=red[tid+s]; __syncthreads(); }
  l=red[0]; float inv=1.f/l;
  const u32x4* Vp=(const u32x4*)(V+(size_t)bh*SK*DH);
  float acc[8]={0};
  for(int k=r;k<SK;k+=16){
    u32x4 v=Vp[k*16+c]; float p=sc[k]*inv; if(MIMIC) p=rbf(p);
    for(int j=0;j<4;j++){ acc[2*j]+=p*lo(v[j]); acc[2*j+1]+=p*hi(v[j]); }
  }
  for(int j=0;j<8;j++) oacc[r][8*c+j]=acc[j];
  __syncthreads();
  if(tid<DH){ float s=0; for(int i=0;i<16;i++) s+=oacc[i][tid]; o[b*H*DH+h*DH+tid]=MIMIC?rbf(s):s; }
}
__global__ void k_out(const u16* x,const float* o,const float* hbuf,const u16* wo,const u16* w2,const u16* b2,u16* out){
  __shared__ float ra[4][64], rm[4][64];
  int b=blockIdx.x, col=blockIdx.y*64+(threadIdx.x&63), g=threadIdx.x>>6;
  float a=0; for(int k=g;k<H*DH;k+=4) a+=o[b*H*DH+k]*bf2f(wo[k*D+col]);
  float m=0; for(int k=g;k<F;k+=4) m+=hbuf[b*F+k]*bf2f(w2[k*D+col]);
  ra[g][threadIdx.x&63]=a; rm[g][threadIdx.x&63]=m; __syncthreads();
  if(g==0){ int t=threadIdx.x; a=ra[0][t]+ra[1][t]+ra[2][t]+ra[3][t]; m=rm[0][t]+rm[1][t]+rm[2][t]+rm[3][t];
    float r;
    if(MIMIC) r=rbf(rbf(bf2f(x[b*D+col])+rbf(a))+rbf(rbf(m)+bf2f(b2[col])));
    else r=bf2f(x[b*D+col])+a+m+bf2f(b2[col]);
    out[b*D+col]=f2bf(r); }
}
extern "C" void kernel_launch(void* const* d_in, const int* in_sizes, int n_in, void* d_out, int out_size, void* d_ws, size_t ws_size, hipStream_t stream){
  const u16* x=(const u16*)d_in[0]; const u16* K=(const u16*)d_in[1]; const u16* V=(const u16*)d_in[2];
  const u16* wq=(const u16*)d_in[3]; const u16* wo=(const u16*)d_in[4];
  const u16* w1=(const u16*)d_in[6]; const u16* b1=(const u16*)d_in[7]; const u16* w2=(const u16*)d_in[8]; const u16* b2=(const u16*)d_in[9];
  float* q=(float*)d_ws; float* hb=q+B*H*DH; float* o=hb+B*F;
  k_proj<<<(H*DH+F+255)/256,256,0,stream>>>(x,wq,w1,b1,q,hb);
  k_attn<<<B*H,256,0,stream>>>(q,K,V,o);
  k_out<<<dim3(B,D/64),256,0,stream>>>(x,o,hb,wo,w2,b2,(u16*)d_out);
}
```

```cpp
#include <hip/hip_runtime.h>
#include <cstdint>

typedef unsigned short u16;
typedef unsigned u32;
typedef unsigned long long u64;
typedef u32 u32x4 __attribute__((ext_vector_type(4)));
typedef float f32x4 __attribute__((ext_vector_type(4)));
typedef short bf16x8 __attribute__((ext_vector_type(8)));
typedef __bf16 bf16x2 __attribute__((ext_vector_type(2)));
#define GAS __attribute__((address_space(1)))
#define RLX_AGENT __ATOMIC_RELAXED, __HIP_MEMORY_SCOPE_AGENT

#define LOOPBAR 0
#define WNT 0
#define STAMPS 0

constexpr int NB = 8, DM = 512, NH = 32, SK = 4096, DH = 128, FF = 1792;
constexpr int NW = 8, T = 8, NIT = SK / (4 * T * NW);
constexpr int MLPH = FF / 64;
constexpr size_t WS_GRAN = 65536;
constexpr size_t WS_STAMP = 4u << 20;

__device__ __forceinline__ float asf(u32 v) { return __builtin_bit_cast(float, v); }
__device__ __forceinline__ u32 asu(float v) { return __builtin_bit_cast(u32, v); }
__device__ __forceinline__ float blo(u32 v) { return asf(v << 16); }
__device__ __forceinline__ float bhi(u32 v) { return asf(v & 0xffff0000u); }
__device__ __forceinline__ u32 cvtpk(float lo, float hi) {
  bf16x2 r = {(__bf16)lo, (__bf16)hi};
  return __builtin_bit_cast(u32, r);
}
__device__ __forceinline__ float rbf(float x) { return (float)(__bf16)x; }
__device__ __forceinline__ float dot2(u32 a, u32 b, float c) {
  return __builtin_amdgcn_fdot2_f32_bf16(__builtin_bit_cast(bf16x2, a), __builtin_bit_cast(bf16x2, b), c, false);
}
template <int CTRL> __device__ __forceinline__ float dpp(float x) {
  return asf(__builtin_amdgcn_mov_dpp(asu(x), CTRL, 0xf, 0xf, true));
}
constexpr int XOR1 = 0xB1, XOR2 = 0x4E, HMIR = 0x141, ROR8 = 0x128, ROR4 = 0x124;
__device__ __forceinline__ float rowsum16(float x) {
  x += dpp<XOR1>(x); x += dpp<XOR2>(x); x += dpp<HMIR>(x); x += dpp<ROR8>(x); return x;
}
__device__ __forceinline__ float xrow_max(float x) {
  auto s = __builtin_amdgcn_permlane16_swap(asu(x), asu(x), false, false);
  x = fmaxf(asf(s[0]), asf(s[1]));
  auto t = __builtin_amdgcn_permlane32_swap(asu(x), asu(x), false, false);
  return fmaxf(asf(t[0]), asf(t[1]));
}
__device__ __forceinline__ float xrow_sum(float x) {
  auto s = __builtin_amdgcn_permlane16_swap(asu(x), asu(x), false, false);
  x = asf(s[0]) + asf(s[1]);
  auto t = __builtin_amdgcn_permlane32_swap(asu(x), asu(x), false, false);
  return asf(t[0]) + asf(t[1]);
}
__device__ __forceinline__ f32x4 mfma(u32x4 a, u32x4 b, f32x4 c) {
  return __builtin_amdgcn_mfma_f32_16x16x32_bf16(__builtin_bit_cast(bf16x8, a), __builtin_bit_cast(bf16x8, b), c, 0, 0, 0);
}
__device__ __forceinline__ u32x4 ldnt(const u32x4* p) { return __builtin_nontemporal_load(p); }
__device__ __forceinline__ u32x4 ldw(const u32x4* p) { return WNT ? __builtin_nontemporal_load(p) : *p; }
#define SB() __builtin_amdgcn_sched_barrier(0)
__device__ __forceinline__ void lds_barrier() {
  asm volatile("s_waitcnt lgkmcnt(0)\n\ts_barrier" ::: "memory");
}

struct Lds {
  float qpart[NW][DH];
  float hpart[2 * NW][64];
  float orec[NW][DH];
  float ml[2][NW];
  float ypart[NW][DM];
};

__global__ __launch_bounds__(NW * 64, 2) void fused_decode(
    const u16* __restrict__ x, const u16* __restrict__ Kc, const u16* __restrict__ Vc, const u16* __restrict__ wq,
    const u16* __restrict__ wo, const u16* __restrict__ w1, const u16* __restrict__ b1, const u16* __restrict__ w2,
    const u16* __restrict__ b2, u16* __restrict__ out, unsigned char* __restrict__ ws) {
  __shared__ __attribute__((aligned(16))) Lds lds;
  const int tid = threadIdx.x, lane = tid & 63, w = __builtin_amdgcn_readfirstlane(tid >> 6);
  const int g = lane >> 4, c = lane & 15;
  const int bid = blockIdx.x, h = (bid & 7) * 4 + ((bid >> 3) & 3), b = bid >> 5;
  const bool has_mlp = h < MLPH;

  GAS u32* ep = (GAS u32*)(ws + 256 * b);
  const u32 epoch = __hip_atomic_load(ep, RLX_AGENT);
  const u32x4* xr = (const u32x4*)(x + b * DM);
  const u32x4 x8 = xr[lane];
  const u32x4 xa = xr[8 * w + 2 * g], xb = xr[8 * w + 2 * g + 1];
  u32x4 wr[16];
  {
    const u32x4* p = (const u32x4*)(wq + (size_t)(64 * w + 16 * g) * (NH * DH) + h * DH) + c;
#pragma unroll
    for (int i = 0; i < 16; i++) wr[i] = ldw(p + i * (NH * DH / 8));
  }
  const size_t bh = (size_t)(b * NH + h) * (SK * DH / 8);
  const u32x4* kb = (const u32x4*)Kc + bh + w * (64 * T) + lane;
  const u32x4* vb = (const u32x4*)Vc + bh + w * (64 * T) + lane;
  u32x4 k[T], v[T];
#pragma unroll
  for (int t = 0; t < T; t++) k[t] = ldnt(kb + t * 64);
#pragma unroll
  for (int t = 0; t < T; t++) v[t] = ldnt(vb + t * 64);
  SB();

  u32x4 m4, m8;
#pragma unroll
  for (int r = 0; r < 4; r++) {
    const u32 mk = (((c & 7) >> 1) == r) ? ((c & 1) ? 0xffff0000u : 0x0000ffffu) : 0u;
    m8[r] = mk; m4[r] = c < 8 ? mk : 0u;
  }

  GAS u64* myslot = (GAS u64*)(ws + WS_GRAN) + ((size_t)(b * 32 + (tid >> 4)) * 32 + h) * 16 + (tid & 15);
  __hip_atomic_store(myslot, (u64)epoch << 32, RLX_AGENT);

  u32 xn2[16];
  {
    float s = 0.f;
#pragma unroll
    for (int j = 0; j < 4; j++) s += blo(x8[j]) + bhi(x8[j]);
    s = xrow_sum(rowsum16(s));
    const float mean = s * (1.f / DM);
    float q = 0.f;
#pragma unroll
    for (int j = 0; j < 4; j++) { const float d0 = blo(x8[j]) - mean, d1 = bhi(x8[j]) - mean; q = fmaf(d0, d0, q); q = fmaf(d1, d1, q); }
    q = xrow_sum(rowsum16(q));
    const float mb = rbf(mean), den = rbf(sqrtf(rbf(rbf(q * (1.f / DM)) + 1e-5f)));
    const float r = 1.f / den;
#pragma unroll
    for (int i = 0; i < 16; i++) {
      const u32 wd = i < 8 ? (u32)xa[(i >> 1) & 3] : (u32)xb[(i >> 1) & 3];
      const float a = rbf(((i & 1) ? bhi(wd) : blo(wd)) - mb);
      const float q0 = a * r, qq = fmaf(fmaf(-q0, den, a), r, q0);
      xn2[i] = cvtpk(qq, qq);
    }
  }
  {
    f32x4 acc = {0.f, 0.f, 0.f, 0.f};
#pragma unroll
    for (int i = 0; i < 16; i++) acc = mfma(m4 & xn2[i], wr[i], acc);
    if (lane < 32) *(f32x4*)&lds.qpart[w][8 * c + 4 * g] = acc;
  }
  SB();
  const int hm = has_mlp ? h : 0;
  u32x4 w1r[8], w2r[8];
  {
    const u32x4* p = (const u32x4*)(w1 + (size_t)(64 * w + 8 * (lane >> 3)) * FF + hm * 64) + (lane & 7);
#pragma unroll
    for (int i = 0; i < 8; i++) w1r[i] = ldw(p + i * (FF / 8));
    const u32x4* p2 = (const u32x4*)(w2 + (size_t)(64 * hm + 8 * w + g) * DM) + c;
#pragma unroll
    for (int i = 0; i < 8; i++) w2r[i] = ldw(p2 + (i & 1) * (4 * DM / 8) + (i >> 1) * 16);
  }
  const float b1v = asf((u32)b1[64 * hm + 8 * w + 4 * (c & 1) + g] << 16);
  SB();
  lds_barrier();
  u32x4 qp;
  {
    f32x4 a = *(const f32x4*)&lds.qpart[0][8 * c], bq = *(const f32x4*)&lds.qpart[0][8 * c + 4];
#pragma unroll
    for (int i = 1; i < NW; i++) { a += *(const f32x4*)&lds.qpart[i][8 * c]; bq += *(const f32x4*)&lds.qpart[i][8 * c + 4]; }
    qp[0] = cvtpk(a[0], a[1]); qp[1] = cvtpk(a[2], a[3]); qp[2] = cvtpk(bq[0], bq[1]); qp[3] = cvtpk(bq[2], bq[3]);
  }

  float m = -INFINITY, l = 0.f;
  f32x4 oacc = {0.f, 0.f, 0.f, 0.f};
  f32x4 y[4];
  const u32x4* wop = (const u32x4*)(wo + (size_t)(h * DH + 16 * w + g) * DM) + c;
  constexpr float SCALE = 0.08838834764831845f, L2E = 1.4426950408889634f;

  auto tile = [&](auto nextk, auto nextv) {
    float s[T];
#pragma unroll
    for (int t = 0; t < T; t++) {
      float d = dot2(k[t][0], qp[0], 0.f);
      d = dot2(k[t][1], qp[1], d); d = dot2(k[t][2], qp[2], d); d = dot2(k[t][3], qp[3], d);
      s[t] = d;
      k[t] = nextk(t);
      SB();
    }
    float tm = -INFINITY;
#pragma unroll
    for (int t = 0; t < T; t++) { s[t] = rbf(rbf(rowsum16(s[t])) * SCALE) * L2E; tm = fmaxf(tm, s[t]); }
    tm = xrow_max(tm);
    const float mn = fmaxf(m, tm), alpha = __builtin_amdgcn_exp2f(m - mn);
    m = mn;
    float ps = 0.f;
#pragma unroll
    for (int t = 0; t < T; t++) { s[t] = __builtin_amdgcn_exp2f(s[t] - mn); ps += s[t]; }
    l = fmaf(l, alpha, ps);
    oacc *= alpha;
    SB();
#pragma unroll
    for (int t = 0; t < T; t++) {
      oacc = mfma(m4 & cvtpk(s[t], s[t]), v[t], oacc);
      v[t] = nextv(t);
      SB();
    }
  };

  tile([&](int t) { return ldnt(kb + (64 * T * NW) + t * 64); }, [&](int t) { return ldnt(vb + (64 * T * NW) + t * 64); });
  {
    f32x4 acc = {0.f, 0.f, 0.f, 0.f};
#pragma unroll
    for (int i = 0; i < 8; i++) { const u32 e0 = xn2[i], e1 = xn2[8 + i]; acc = mfma(m8 & (c < 8 ? e0 : e1), w1r[i], acc); }
    if ((g >> 1) == (c >> 3)) *(f32x4*)&lds.hpart[2 * w + (g >> 1)][8 * (c & 7) + 4 * (g & 1)] = acc;
    lds_barrier();
    const int u = 8 * w + 4 * (c & 1) + g;
    float hs = lds.hpart[2 * (c >> 1)][u] + lds.hpart[2 * (c >> 1) + 1][u];
    hs += dpp<XOR2>(hs); hs += dpp<ROR4>(hs); hs += dpp<ROR8>(hs);
    const float hv = rbf(rbf(hs) + b1v);
    const float e = __builtin_amdgcn_exp2f((2.f * L2E * 0.7978845608028654f) * (hv + 0.044715f * hv * hv * hv));
    const float th = 1.f - 2.f / (e + 1.f);
    float gl = 0.5f * hv * (1.f + th);
    gl = has_mlp ? gl : 0.f;
    const u32 g2 = cvtpk(gl, gl);
    const u32 ga = __builtin_amdgcn_mov_dpp(g2, 0x00, 0xf, 0xf, true), gb = __builtin_amdgcn_mov_dpp(g2, 0x55, 0xf, 0xf, true);
#pragma unroll
    for (int cb = 0; cb < 4; cb++) {
      f32x4 a = {0.f, 0.f, 0.f, 0.f};
      a = mfma(m4 & ga, w2r[2 * cb], a);
      y[cb] = mfma(m4 & gb, w2r[2 * cb + 1], a);
    }
  }
#pragma unroll 1
  for (int it = 2; it < NIT; it++) {
    const int off = it * (64 * T * NW);
    tile([&](int t) { return ldnt(kb + off + t * 64); }, [&](int t) { return ldnt(vb + off + t * 64); });
  }
  tile([&](int t) { return ldw(wop + (t & 3) * (4 * DM / 8) + (t >> 2) * 16); },
       [&](int t) { return ldw(wop + (t & 3) * (4 * DM / 8) + (2 + (t >> 2)) * 16); });

  l = xrow_sum(l);
  if (lane < 32) *(f32x4*)&lds.orec[w][8 * c + 4 * g] = oacc;
  if (lane == 0) { lds.ml[0][w] = m; lds.ml[1][w] = l; }
  lds_barrier();
  {
    const f32x4 ma = *(const f32x4*)&lds.ml[0][0], mb = *(const f32x4*)&lds.ml[0][4];
    const f32x4 la = *(const f32x4*)&lds.ml[1][0], lb = *(const f32x4*)&lds.ml[1][4];
    const int d = 16 * w + 4 * (c & 3) + g, r0 = 2 * (c >> 2);
    const float o0 = lds.orec[r0][d], o1 = lds.orec[r0 + 1][d];
    const float m0 = lds.ml[0][r0], m1 = lds.ml[0][r0 + 1];
    const float M = fmaxf(fmaxf(fmaxf(ma[0], ma[1]), fmaxf(ma[2], ma[3])), fmaxf(fmaxf(mb[0], mb[1]), fmaxf(mb[2], mb[3])));
    float L = 0.f;
#pragma unroll
    for (int i = 0; i < 4; i++) { L = fmaf(__builtin_amdgcn_exp2f(ma[i] - M), la[i], L); L = fmaf(__builtin_amdgcn_exp2f(mb[i] - M), lb[i], L); }
    float o = __builtin_amdgcn_exp2f(m0 - M) * o0 + __builtin_amdgcn_exp2f(m1 - M) * o1;
    o += dpp<ROR4>(o); o += dpp<ROR8>(o);
    o = o / L;
    const u32 o2 = cvtpk(o, o);
    u32 oq[4];
    oq[0] = __builtin_amdgcn_mov_dpp(o2, 0x00, 0xf, 0xf, true); oq[1] = __builtin_amdgcn_mov_dpp(o2, 0x55, 0xf, 0xf, true);
    oq[2] = __builtin_amdgcn_mov_dpp(o2, 0xAA, 0xf, 0xf, true); oq[3] = __builtin_amdgcn_mov_dpp(o2, 0xFF, 0xf, 0xf, true);
#pragma unroll
    for (int t = 0; t < 8; t++) y[t >> 2] = mfma(m4 & oq[t & 3], k[t], y[t >> 2]);
#pragma unroll
    for (int t = 0; t < 8; t++) y[2 + (t >> 2)] = mfma(m4 & oq[t & 3], v[t], y[2 + (t >> 2)]);
    if (lane < 32) {
#pragma unroll
      for (int cb = 0; cb < 4; cb++) *(f32x4*)&lds.ypart[w][128 * cb + 8 * c + 4 * g] = y[cb];
    }
  }
  lds_barrier();
  {
    float s = lds.ypart[0][tid];
#pragma unroll
    for (int i = 1; i < NW; i++) s += lds.ypart[i][tid];
    __hip_atomic_store(myslot, ((u64)(epoch + 1u) << 32) | asu(s), RLX_AGENT);
  }
  if (w == 0) {
    const float base = asf((u32)x[b * DM + 16 * h + c] << 16) + asf((u32)b2[16 * h + c] << 16);
    GAS u64* src = (GAS u64*)(ws + WS_GRAN) + ((size_t)(b * 32 + h) * 32 + g) * 16 + c;
    u64 gv[8];
    for (u32 spins = 0;; spins++) {
      bool ok = true;
#pragma unroll
      for (int i = 0; i < 8; i++) { gv[i] = __hip_atomic_load(src + i * 64, RLX_AGENT); ok &= (u32)(gv[i] >> 32) == epoch + 1u; }
      if (__all(ok) || spins > (1u << 20)) break;
      __builtin_amdgcn_s_sleep(8);
    }
    float s = 0.f;
#pragma unroll
    for (int i = 0; i < 8; i++) s += asf((u32)gv[i]);
    s = xrow_sum(s) + base;
    if (lane < 16) out[b * DM + 16 * h + c] = (u16)(cvtpk(s, s) & 0xffffu);
    if (h == 0 && lane == 0) __hip_atomic_store(ep, epoch + 1u, RLX_AGENT);
  }
}

extern "C" void kernel_launch(void* const* d_in, const int* in_sizes, int n_in, void* d_out, int out_size, void* d_ws,
                              size_t ws_size, hipStream_t stream) {
  fused_decode<<<NB * NH, NW * 64, 0, stream>>>((const u16*)d_in[0], (const u16*)d_in[1], (const u16*)d_in[2], (const u16*)d_in[3],
                                                (const u16*)d_in[4], (const u16*)d_in[6], (const u16*)d_in[7], (const u16*)d_in[8],
                                                (const u16*)d_in[9], (u16*)d_out, (unsigned char*)d_ws);
}
```

```cpp
#include <hip/hip_runtime.h>
#include <cstdint>

typedef unsigned short u16;
typedef unsigned u32;
typedef unsigned long long u64;
typedef u32 u32x4 __attribute__((ext_vector_type(4)));
typedef float f32x4 __attribute__((ext_vector_type(4)));
typedef short bf16x8 __attribute__((ext_vector_type(8)));
typedef __bf16 bf16x2 __attribute__((ext_vector_type(2)));
typedef __amdgpu_buffer_rsrc_t rsrc_t;
#define GAS __attribute__((address_space(1)))
#define RLX_AGENT __ATOMIC_RELAXED, __HIP_MEMORY_SCOPE_AGENT

#define LOOPBAR 0
#define WAUX 0
#define STAMPS 0

constexpr int NB = 8, DM = 512, NH = 32, SK = 4096, DH = 128, FF = 1792;
constexpr int NW = 8, T = 8, NIT = SK / (4 * T * NW);
constexpr int TILE_STRIDE = NW * T * 1024;
constexpr int MLPH = FF / 64;
constexpr size_t WS_GRAN = 65536;
constexpr size_t WS_STAMP = 4u << 20;

__device__ __forceinline__ float asf(u32 v) { return __builtin_bit_cast(float, v); }
__device__ __forceinline__ u32 asu(float v) { return __builtin_bit_cast(u32, v); }
__device__ __forceinline__ float blo(u32 v) { return asf(v << 16); }
__device__ __forceinline__ float bhi(u32 v) { return asf(v & 0xffff0000u); }
__device__ __forceinline__ u32 cvtpk(float lo, float hi) {
  bf16x2 r = {(__bf16)lo, (__bf16)hi};
  return __builtin_bit_cast(u32, r);
}
__device__ __forceinline__ float rbf(float x) { return (float)(__bf16)x; }
__device__ __forceinline__ float dot2(u32 a, u32 b, float c) {
  return __builtin_amdgcn_fdot2_f32_bf16(__builtin_bit_cast(bf16x2, a), __builtin_bit_cast(bf16x2, b), c, false);
}
template <int CTRL> __device__ __forceinline__ float dpp(float x) {
  return asf(__builtin_amdgcn_mov_dpp(asu(x), CTRL, 0xf, 0xf, true));
}
template <int CTRL> __device__ __forceinline__ u32 dppu(u32 x) { return __builtin_amdgcn_mov_dpp(x, CTRL, 0xf, 0xf, true); }
constexpr int XOR1 = 0xB1, XOR2 = 0x4E, HMIR = 0x141, ROR8 = 0x128, ROR4 = 0x124;
__device__ __forceinline__ float rowsum16(float x) {
  x += dpp<XOR1>(x); x += dpp<XOR2>(x); x += dpp<HMIR>(x); x += dpp<ROR8>(x); return x;
}
__device__ __forceinline__ float xrow_max(float x) {
  auto s = __builtin_amdgcn_permlane16_swap(asu(x), asu(x), false, false);
  x = fmaxf(asf(s[0]), asf(s[1]));
  auto t = __builtin_amdgcn_permlane32_swap(asu(x), asu(x), false, false);
  return fmaxf(asf(t[0]), asf(t[1]));
}
__device__ __forceinline__ float xrow_sum(float x) {
  auto s = __builtin_amdgcn_permlane16_swap(asu(x), asu(x), false, false);
  x = asf(s[0]) + asf(s[1]);
  auto t = __builtin_amdgcn_permlane32_swap(asu(x), asu(x), false, false);
  return asf(t[0]) + asf(t[1]);
}
__device__ __forceinline__ f32x4 mfma(u32x4 a, u32x4 b, f32x4 c) {
  return __builtin_amdgcn_mfma_f32_16x16x32_bf16(__builtin_bit_cast(bf16x8, a), __builtin_bit_cast(bf16x8, b), c, 0, 0, 0);
}
__device__ __forceinline__ rsrc_t mkrsrc(const void* p, u32 bytes) {
  return __builtin_amdgcn_make_buffer_rsrc((void*)p, 0, bytes, 0x00020000);
}
template <int AUX> __device__ __forceinline__ u32x4 bld(rsrc_t r, u32 voff, u32 soff) {
  return __builtin_amdgcn_raw_buffer_load_b128(r, voff, soff, AUX);
}
#define SB() __builtin_amdgcn_sched_barrier(0)
#define PIN(x) asm volatile("" : "+v"(x))
__device__ __forceinline__ void lds_barrier() {
  asm volatile("s_waitcnt lgkmcnt(0)\n\ts_barrier" ::: "memory");
}
template <int N> struct IC { static constexpr int v = N; };

struct Lds {
  float qpart[NW][DH];
  float hpart[2 * NW][64];
  float orec[NW][DH];
  float ml[2][NW];
  float ypart[NW][DM];
};

__global__ __launch_bounds__(NW * 64, 2) void fused_decode(
    const u16* __restrict__ x, const u16* __restrict__ Kc, const u16* __restrict__ Vc, const u16* __restrict__ wq,
    const u16* __restrict__ wo, const u16* __restrict__ w1, const u16* __restrict__ b1, const u16* __restrict__ w2,
    const u16* __restrict__ b2, u16* __restrict__ out, unsigned char* __restrict__ ws) {
  __shared__ __attribute__((aligned(16))) Lds lds;
  const int tid = threadIdx.x, lane = tid & 63, w = __builtin_amdgcn_readfirstlane(tid >> 6);
  const int g = lane >> 4, c = lane & 15;
  const int bid = blockIdx.x, h = (bid & 7) * 4 + ((bid >> 3) & 3), b = bid >> 5;
  const bool has_mlp = h < MLPH;
  const int hm = has_mlp ? h : 0;

  const size_t bh = (size_t)(b * NH + h) * (SK * DH);
  const rsrc_t rk = mkrsrc(Kc + bh, SK * DH * 2), rv = mkrsrc(Vc + bh, SK * DH * 2);
  const rsrc_t rx = mkrsrc(x + b * DM, DM * 2);
  const rsrc_t rq = mkrsrc(wq + h * DH, (DM * NH * DH - h * DH) * 2);
  const u32 l16 = lane * 16, l16b = l16 + 4096;
  const u32 kw = w * (T * 1024);
  auto ldk = [&](int (tile), int t) { return bld<2>(rk, (t < 4 ? l16 : l16b) + (t & 3) * 1024, kw + tile * TILE_STRIDE); };
  auto ldv = [&](int (tile), int t) { return bld<2>(rv, (t < 4 ? l16 : l16b) + (t & 3) * 1024, kw + tile * TILE_STRIDE); };
  const u32 qoff = g * (16 * NH * DH * 2) + c * 16;
  auto ldq = [&](int i) { return bld<WAUX>(rq, qoff, (64 * w + i) * (NH * DH * 2)); };

  u32x4 ka[T], va[T], kb[T], vb[T], wr[16];
  ka[0] = ldk(0, 0); ka[1] = ldk(0, 1);
  const u32x4 x8 = bld<0>(rx, l16, 0);
  const u32x4 xa = bld<0>(rx, g * 32, w * 128), xb = bld<0>(rx, g * 32 + 16, w * 128);
  GAS u32* ep = (GAS u32*)(ws + 256 * b);
  const u32 epoch = __hip_atomic_load(ep, RLX_AGENT);
  SB();
  wr[0] = ldq(0); ka[2] = ldk(0, 2); wr[1] = ldq(1); ka[3] = ldk(0, 3); SB();
  wr[2] = ldq(2); ka[4] = ldk(0, 4); wr[3] = ldq(3); ka[5] = ldk(0, 5); SB();

  u32x4 m4, m8;
#pragma unroll
  for (int r = 0; r < 4; r++) {
    const u32 mk = (((c & 7) >> 1) == r) ? ((c & 1) ? 0xffff0000u : 0x0000ffffu) : 0u;
    m8[r] = mk; m4[r] = c < 8 ? mk : 0u;
  }
  u32 xn2[16];
  {
    float s = 0.f;
#pragma unroll
    for (int j = 0; j < 4; j++) s += blo(x8[j]) + bhi(x8[j]);
    s = xrow_sum(rowsum16(s));
    const float mean = s * (1.f / DM);
    float q = 0.f;
#pragma unroll
    for (int j = 0; j < 4; j++) { const float d0 = blo(x8[j]) - mean, d1 = bhi(x8[j]) - mean; q = fmaf(d0, d0, q); q = fmaf(d1, d1, q); }
    q = xrow_sum(rowsum16(q));
    const float mb = rbf(mean), den = rbf(sqrtf(rbf(rbf(q * (1.f / DM)) + 1e-5f)));
    const float r = 1.f / den;
#pragma unroll
    for (int i = 0; i < 16; i++) {
      const u32 wd = i < 8 ? (u32)xa[(i >> 1) & 3] : (u32)xb[(i >> 1) & 3];
      const float a = rbf(((i & 1) ? bhi(wd) : blo(wd)) - mb);
      const float q0 = a * r, qq = fmaf(fmaf(-q0, den, a), r, q0);
      xn2[i] = cvtpk(qq, qq);
      PIN(xn2[i]);
    }
  }
  SB();
  GAS u64* myslot = (GAS u64*)(ws + WS_GRAN) + ((size_t)(b * 32 + (tid >> 4)) * 32 + h) * 16 + (tid & 15);
  __hip_atomic_store(myslot, (u64)epoch << 32, RLX_AGENT);
  ka[6] = ldk(0, 6); wr[4] = ldq(4); ka[7] = ldk(0, 7); wr[5] = ldq(5); va[0] = ldv(0, 0); SB();
  va[1] = ldv(0, 1); wr[6] = ldq(6); va[2] = ldv(0, 2); wr[7] = ldq(7); va[3] = ldv(0, 3); SB();
  va[4] = ldv(0, 4); wr[8] = ldq(8); va[5] = ldv(0, 5); wr[9] = ldq(9); va[6] = ldv(0, 6); SB();
  va[7] = ldv(0, 7); wr[10] = ldq(10); kb[0] = ldk(1, 0); wr[11] = ldq(11); kb[1] = ldk(1, 1); SB();
  kb[2] = ldk(1, 2); wr[12] = ldq(12); kb[3] = ldk(1, 3); wr[13] = ldq(13); kb[4] = ldk(1, 4); SB();
  kb[5] = ldk(1, 5); wr[14] = ldq(14); kb[6] = ldk(1, 6); wr[15] = ldq(15); SB();
  __builtin_amdgcn_s_barrier();
  kb[7] = ldk(1, 7);
#pragma unroll
  for (int t = 0; t < T; t++) vb[t] = ldv(1, t);
  SB();
  {
    f32x4 acc = {0.f, 0.f, 0.f, 0.f};
#pragma unroll
    for (int i = 0; i < 16; i++) acc = mfma(m4 & xn2[i], wr[i], acc);
    if (lane < 32) *(f32x4*)&lds.qpart[w][8 * c + 4 * g] = acc;
  }
  u32 xn1[8];
#pragma unroll
  for (int i = 0; i < 8; i++) { const u32 e0 = xn2[i], e1 = xn2[8 + i]; xn1[i] = c < 8 ? e0 : e1; PIN(xn1[i]); }
  lds_barrier();
  u32x4 qp;
  {
    f32x4 a = *(const f32x4*)&lds.qpart[0][8 * c], bq = *(const f32x4*)&lds.qpart[0][8 * c + 4];
#pragma unroll
    for (int i = 1; i < NW; i++) { a += *(const f32x4*)&lds.qpart[i][8 * c]; bq += *(const f32x4*)&lds.qpart[i][8 * c + 4]; }
    qp[0] = cvtpk(a[0], a[1]); qp[1] = cvtpk(a[2], a[3]); qp[2] = cvtpk(bq[0], bq[1]); qp[3] = cvtpk(bq[2], bq[3]);
  }

  const rsrc_t r1 = mkrsrc(w1 + hm * 64, (DM * FF - hm * 64) * 2);
  const rsrc_t r2 = mkrsrc(w2 + (size_t)hm * 64 * DM, 64 * DM * 2);
  const rsrc_t ro = mkrsrc(wo + (size_t)h * DH * DM, DH * DM * 2);
  const u32 off1 = (lane >> 3) * (8 * FF * 2) + (lane & 7) * 16, off2 = g * (DM * 2) + c * 16;
  auto side = [&](auto E) {
    constexpr int e = decltype(E)::v;
    if constexpr (e < 0) {
    } else if constexpr (e < 8) wr[e] = bld<WAUX>(r1, off1, (64 * w + e) * (FF * 2));
    else if constexpr (e < 16) wr[e] = bld<WAUX>(r2, off2 + ((e - 8) >> 1) * 256, (8 * w + 4 * (e & 1)) * (DM * 2));
    else if constexpr (e < 24) kb[e - 16] = bld<WAUX>(ro, off2 + ((e - 16) >> 2) * 256, (16 * w + 4 * (e & 3)) * (DM * 2));
    else vb[e - 24] = bld<WAUX>(ro, off2 + ((e - 16) >> 2) * 256, (16 * w + 4 * (e & 3)) * (DM * 2));
  };

  float m = -INFINITY, l = 0.f;
  f32x4 oacc = {0.f, 0.f, 0.f, 0.f};
  f32x4 y[4];
  constexpr float SCALE = 0.08838834764831845f, L2E = 1.4426950408889634f;

  auto tile = [&](u32x4 (&k)[T], u32x4 (&v)[T], int nx, auto REFILL, auto E0) {
    constexpr bool refill = decltype(REFILL)::v;
    constexpr int e0 = decltype(E0)::v;
    float s[T];
#pragma unroll
    for (int t = 0; t < T; t++) {
      float d = dot2(k[t][0], qp[0], 0.f);
      d = dot2(k[t][1], qp[1], d); d = dot2(k[t][2], qp[2], d); d = dot2(k[t][3], qp[3], d);
      PIN(d);
      s[t] = d;
      if constexpr (refill) k[t] = ldk(nx, t);
      if (t == 3) side(IC<e0>{});
      if (t == 7) side(IC<(e0 < 0 ? -1 : e0 + 1)>{});
      SB();
    }
    float tm = -INFINITY;
#pragma unroll
    for (int t = 0; t < T; t++) { s[t] = rbf(rbf(rowsum16(s[t])) * SCALE) * L2E; tm = fmaxf(tm, s[t]); }
    tm = xrow_max(tm);
    const float mn = fmaxf(m, tm), alpha = __builtin_amdgcn_exp2f(m - mn);
    m = mn;
    float ps = 0.f;
#pragma unroll
    for (int t = 0; t < T; t++) { s[t] = __builtin_amdgcn_exp2f(s[t] - mn); ps += s[t]; }
    l = fmaf(l, alpha, ps);
    oacc *= alpha;
    SB();
#pragma unroll
    for (int t = 0; t < T; t++) {
      oacc = mfma(m4 & cvtpk(s[t], s[t]), v[t], oacc);
      PIN(oacc);
      if constexpr (refill) v[t] = ldv(nx, t);
      if (t == 3) side(IC<(e0 < 0 ? -1 : e0 + 2)>{});
      if (t == 7) side(IC<(e0 < 0 ? -1 : e0 + 3)>{});
      SB();
    }
  };

  tile(ka, va, 2, IC<1>{}, IC<0>{});
  tile(kb, vb, 0, IC<0>{}, IC<-1>{});
  tile(ka, va, 3, IC<1>{}, IC<4>{});
  tile(ka, va, 4, IC<1>{}, IC<8>{});
  const float b1v = asf((u32)b1[64 * hm + 8 * w + 4 * (c & 1) + g] << 16);
  tile(ka, va, 5, IC<1>{}, IC<12>{});
  {
    f32x4 acc = {0.f, 0.f, 0.f, 0.f};
#pragma unroll
    for (int i = 0; i < 8; i++) acc = mfma(m8 & xn1[i], wr[i], acc);
    if ((g >> 1) == (c >> 3)) *(f32x4*)&lds.hpart[2 * w + (g >> 1)][8 * (c & 7) + 4 * (g & 1)] = acc;
  }
  tile(ka, va, 6, IC<1>{}, IC<16>{});
  {
    lds_barrier();
    const int u = 8 * w + 4 * (c & 1) + g;
    float hs = lds.hpart[2 * (c >> 1)][u] + lds.hpart[2 * (c >> 1) + 1][u];
    hs += dpp<XOR2>(hs); hs += dpp<ROR4>(hs); hs += dpp<ROR8>(hs);
    const float hv = rbf(rbf(hs) + b1v);
    const float e = __builtin_amdgcn_exp2f((2.f * L2E * 0.7978845608028654f) * (hv + 0.044715f * hv * hv * hv));
    const float th = 1.f - 2.f / (e + 1.f);
    float gl = 0.5f * hv * (1.f + th);
    gl = has_mlp ? gl : 0.f;
    const u32 g2 = cvtpk(gl, gl);
    const u32 ga = dppu<0x00>(g2), gb = dppu<0x55>(g2);
#pragma unroll
    for (int cb = 0; cb < 4; cb++) {
      f32x4 a = {0.f, 0.f, 0.f, 0.f};
      a = mfma(m4 & ga, wr[8 + 2 * cb], a);
      y[cb] = mfma(m4 & gb, wr[9 + 2 * cb], a);
      PIN(y[cb]);
    }
  }
  tile(ka, va, 7, IC<1>{}, IC<20>{});
  tile(ka, va, 8, IC<1>{}, IC<24>{});
  tile(ka, va, 9, IC<1>{}, IC<28>{});
#pragma unroll 1
  for (int it = 10; it < NIT; it++) {
    tile(ka, va, it, IC<1>{}, IC<-1>{});
  }
  tile(ka, va, 0, IC<0>{}, IC<-1>{});

  l = xrow_sum(l);
  if (lane < 32) *(f32x4*)&lds.orec[w][8 * c + 4 * g] = oacc;
  if (lane == 0) { lds.ml[0][w] = m; lds.ml[1][w] = l; }
  lds_barrier();
  {
    const f32x4 ma = *(const f32x4*)&lds.ml[0][0], mb = *(const f32x4*)&lds.ml[0][4];
    const f32x4 la = *(const f32x4*)&lds.ml[1][0], lb = *(const f32x4*)&lds.ml[1][4];
    const int d = 16 * w + 4 * (c & 3) + g, r0 = 2 * (c >> 2);
    const float o0 = lds.orec[r0][d], o1 = lds.orec[r0 + 1][d];
    const float m0 = lds.ml[0][r0], m1 = lds.ml[0][r0 + 1];
    const float M = fmaxf(fmaxf(fmaxf(ma[0], ma[1]), fmaxf(ma[2], ma[3])), fmaxf(fmaxf(mb[0], mb[1]), fmaxf(mb[2], mb[3])));
    float L = 0.f;
#pragma unroll
    for (int i = 0; i < 4; i++) { L = fmaf(__builtin_amdgcn_exp2f(ma[i] - M), la[i], L); L = fmaf(__builtin_amdgcn_exp2f(mb[i] - M), lb[i], L); }
    float o = __builtin_amdgcn_exp2f(m0 - M) * o0 + __builtin_amdgcn_exp2f(m1 - M) * o1;
    o += dpp<ROR4>(o); o += dpp<ROR8>(o);
    o = o / L;
    const u32 o2 = cvtpk(o, o);
    u32 oq[4];
    oq[0] = dppu<0x00>(o2); oq[1] = dppu<0x55>(o2); oq[2] = dppu<0xAA>(o2); oq[3] = dppu<0xFF>(o2);
#pragma unroll
    for (int t = 0; t < 8; t++) y[t >> 2] = mfma(m4 & oq[t & 3], kb[t], y[t >> 2]);
#pragma unroll
    for (int t = 0; t < 8; t++) y[2 + (t >> 2)] = mfma(m4 & oq[t & 3], vb[t], y[2 + (t >> 2)]);
    if (lane < 32) {
#pragma unroll
      for (int cb = 0; cb < 4; cb++) *(f32x4*)&lds.ypart[w][128 * cb + 8 * c + 4 * g] = y[cb];
    }
  }
  lds_barrier();
  {
    float s = lds.ypart[0][tid];
#pragma unroll
    for (int i = 1; i < NW; i++) s += lds.ypart[i][tid];
    __hip_atomic_store(myslot, ((u64)(epoch + 1u) << 32) | asu(s), RLX_AGENT);
  }
  if (w == 0) {
    const float base = asf((u32)x[b * DM + 16 * h + c] << 16) + asf((u32)b2[16 * h + c] << 16);
    GAS u64* src = (GAS u64*)(ws + WS_GRAN) + ((size_t)(b * 32 + h) * 32 + g) * 16 + c;
    u64 gv[8];
    for (u32 spins = 0;; spins++) {
      bool ok = true;
#pragma unroll
      for (int i = 0; i < 8; i++) { gv[i] = __hip_atomic_load(src + i * 64, RLX_AGENT); ok &= (u32)(gv[i] >> 32) == epoch + 1u; }
      if (__all(ok) || spins > (1u << 20)) break;
      __builtin_amdgcn_s_sleep(8);
    }
    float s = 0.f;
#pragma unroll
    for (int i = 0; i < 8; i++) s += asf((u32)gv[i]);
    s = xrow_sum(s) + base;
    if (lane < 16) out[b * DM + 16 * h + c] = (u16)(cvtpk(s, s) & 0xffffu);
    if (h == 0 && lane == 0) __hip_atomic_store(ep, epoch + 1u, RLX_AGENT);
  }
}

extern "C" void kernel_launch(void* const* d_in, const int* in_sizes, int n_in, void* d_out, int out_size, void* d_ws,
                              size_t ws_size, hipStream_t stream) {
  fused_decode<<<NB * NH, NW * 64, 0, stream>>>((const u16*)d_in[0], (const u16*)d_in[1], (const u16*)d_in[2], (const u16*)d_in[3],
                                                (const u16*)d_in[4], (const u16*)d_in[6], (const u16*)d_in[7], (const u16*)d_in[8],
                                                (const u16*)d_in[9], (u16*)d_out, (unsigned char*)d_ws);
}
```

```cpp
#include <hip/hip_runtime.h>
#include <cstdint>
#include <cstdio>

typedef unsigned short u16;
typedef unsigned u32;
typedef unsigned long long u64;
typedef u32 u32x4 __attribute__((ext_vector_type(4)));
typedef float f32x4 __attribute__((ext_vector_type(4)));
typedef short bf16x8 __attribute__((ext_vector_type(8)));
typedef __bf16 bf16x2 __attribute__((ext_vector_type(2)));
typedef __amdgpu_buffer_rsrc_t rsrc_t;
#define GAS __attribute__((address_space(1)))
#define RLX_AGENT __ATOMIC_RELAXED, __HIP_MEMORY_SCOPE_AGENT

#define LOOPBAR 0
#define WAUX 0
#define STAMPS 0

constexpr int NB = 8, DM = 512, NH = 32, SK = 4096, DH = 128, FF = 1792;
constexpr int NW = 8, T = 8, NIT = SK / (4 * T * NW);
constexpr int TILE_STRIDE = NW * T * 1024;
constexpr int MLPH = FF / 64;
constexpr size_t WS_GRAN = 65536;
constexpr size_t WS_STAMP = 4u << 20;

__device__ __forceinline__ float asf(u32 v) { return __builtin_bit_cast(float, v); }
__device__ __forceinline__ u32 asu(float v) { return __builtin_bit_cast(u32, v); }
__device__ __forceinline__ float blo(u32 v) { return asf(v << 16); }
__device__ __forceinline__ float bhi(u32 v) { return asf(v & 0xffff0000u); }
__device__ __forceinline__ u32 cvtpk(float lo, float hi) {
  bf16x2 r = {(__bf16)lo, (__bf16)hi};
  return __builtin_bit_cast(u32, r);
}
__device__ __forceinline__ float rbf(float x) { return (float)(__bf16)x; }
__device__ __forceinline__ float dot2(u32 a, u32 b, float c) {
  return __builtin_amdgcn_fdot2_f32_bf16(__builtin_bit_cast(bf16x2, a), __builtin_bit_cast(bf16x2, b), c, false);
}
template <int CTRL> __device__ __forceinline__ float dpp(float x) {
  return asf(__builtin_amdgcn_mov_dpp(asu(x), CTRL, 0xf, 0xf, true));
}
template <int CTRL> __device__ __forceinline__ u32 dppu(u32 x) { return __builtin_amdgcn_mov_dpp(x, CTRL, 0xf, 0xf, true); }
constexpr int XOR1 = 0xB1, XOR2 = 0x4E, HMIR = 0x141, ROR8 = 0x128, ROR4 = 0x124;
__device__ __forceinline__ float rowsum16(float x) {
  x += dpp<XOR1>(x); x += dpp<XOR2>(x); x += dpp<HMIR>(x); x += dpp<ROR8>(x); return x;
}
__device__ __forceinline__ float xrow_max(float x) {
  auto s = __builtin_amdgcn_permlane16_swap(asu(x), asu(x), false, false);
  x = fmaxf(asf(s[0]), asf(s[1]));
  auto t = __builtin_amdgcn_permlane32_swap(asu(x), asu(x), false, false);
  return fmaxf(asf(t[0]), asf(t[1]));
}
__device__ __forceinline__ float xrow_sum(float x) {
  auto s = __builtin_amdgcn_permlane16_swap(asu(x), asu(x), false, false);
  x = asf(s[0]) + asf(s[1]);
  auto t = __builtin_amdgcn_permlane32_swap(asu(x), asu(x), false, false);
  return asf(t[0]) + asf(t[1]);
}
__device__ __forceinline__ f32x4 mfma(u32x4 a, u32x4 b, f32x4 c) {
  return __builtin_amdgcn_mfma_f32_16x16x32_bf16(__builtin_bit_cast(bf16x8, a), __builtin_bit_cast(bf16x8, b), c, 0, 0, 0);
}
__device__ __forceinline__ rsrc_t mkrsrc(const void* p, u32 bytes) {
  return __builtin_amdgcn_make_buffer_rsrc((void*)p, 0, bytes, 0x00020000);
}
template <int AUX> __device__ __forceinline__ u32x4 bld(rsrc_t r, u32 voff, u32 soff) {
  return __builtin_amdgcn_raw_buffer_load_b128(r, voff, soff, AUX);
}
#define SB() __builtin_amdgcn_sched_barrier(0)
#define PIN(x) asm volatile("" : "+v"(x))
__device__ __forceinline__ void lds_barrier() {
  asm volatile("s_waitcnt lgkmcnt(0)\n\ts_barrier" ::: "memory");
}
template <int N> struct IC { static constexpr int v = N; };

struct Lds {
  float qpart[NW][DH];
  float hpart[2 * NW][64];
  float orec[NW][DH];
  float ml[2][NW];
  float ypart[NW][DM];
};

__global__ __launch_bounds__(NW * 64, 2) void fused_decode(
    const u16* __restrict__ x, const u16* __restrict__ Kc, const u16* __restrict__ Vc, const u16* __restrict__ wq,
    const u16* __restrict__ wo, const u16* __restrict__ w1, const u16* __restrict__ b1, const u16* __restrict__ w2,
    const u16* __restrict__ b2, u16* __restrict__ out, unsigned char* __restrict__ ws) {
  __shared__ __attribute__((aligned(16))) Lds lds;
  const int tid = threadIdx.x, lane = tid & 63, w = __builtin_amdgcn_readfirstlane(tid >> 6);
  const int g = lane >> 4, c = lane & 15;
  const int bid = blockIdx.x, h = (bid & 7) * 4 + ((bid >> 3) & 3), b = bid >> 5;
  const bool has_mlp = h < MLPH;
  const int hm = has_mlp ? h : 0;

  const size_t bh = (size_t)(b * NH + h) * (SK * DH);
  const rsrc_t rk = mkrsrc(Kc + bh, SK * DH * 2), rv = mkrsrc(Vc + bh, SK * DH * 2);
  const rsrc_t rx = mkrsrc(x + b * DM, DM * 2);
  const rsrc_t rq = mkrsrc(wq + h * DH, (DM * NH * DH - h * DH) * 2);
  const u32 l16 = lane * 16, l16b = l16 + 4096;
  const u32 kw = w * (T * 1024);
  auto ldk = [&](int (tile), int t) { return bld<2>(rk, (t < 4 ? l16 : l16b) + (t & 3) * 1024, kw + tile * TILE_STRIDE); };
  auto ldv = [&](int (tile), int t) { return bld<2>(rv, (t < 4 ? l16 : l16b) + (t & 3) * 1024, kw + tile * TILE_STRIDE); };
  const u32 qoff = g * (16 * NH * DH * 2) + c * 16;
  auto ldq = [&](int i) { return bld<WAUX>(rq, qoff, (64 * w + i) * (NH * DH * 2)); };

  u32x4 ka[T], va[T], kb[T], vb[T], wr[16];
  ka[0] = ldk(0, 0); ka[1] = ldk(0, 1);
  const u32x4 x8 = bld<0>(rx, l16, 0);
  const u32x4 xa = bld<0>(rx, g * 32, w * 128), xb = bld<0>(rx, g * 32 + 16, w * 128);
  GAS u32* ep = (GAS u32*)(ws + 256 * b);
  const u32 epoch = __hip_atomic_load(ep, RLX_AGENT);
  SB();
  wr[0] = ldq(0); ka[2] = ldk(0, 2); wr[1] = ldq(1); ka[3] = ldk(0, 3); SB();
  wr[2] = ldq(2); ka[4] = ldk(0, 4); wr[3] = ldq(3); ka[5] = ldk(0, 5); SB();

  u32x4 m4, m8;
#pragma unroll
  for (int r = 0; r < 4; r++) {
    const u32 mk = (((c & 7) >> 1) == r) ? ((c & 1) ? 0xffff0000u : 0x0000ffffu) : 0u;
    m8[r] = mk; m4[r] = c < 8 ? mk : 0u;
  }
  u32 xn2[16];
  {
    float s = 0.f;
#pragma unroll
    for (int j = 0; j < 4; j++) s += blo(x8[j]) + bhi(x8[j]);
    s = xrow_sum(rowsum16(s));
    const float mean = s * (1.f / DM);
    float q = 0.f;
#pragma unroll
    for (int j = 0; j < 4; j++) { const float d0 = blo(x8[j]) - mean, d1 = bhi(x8[j]) - mean; q = fmaf(d0, d0, q); q = fmaf(d1, d1, q); }
    q = xrow_sum(rowsum16(q));
    const float mb = rbf(mean), den = rbf(sqrtf(rbf(rbf(q * (1.f / DM)) + 1e-5f)));
    const float r = 1.f / den;
#pragma unroll
    for (int i = 0; i < 16; i++) {
      const u32 wd = i < 8 ? (u32)xa[(i >> 1) & 3] : (u32)xb[(i >> 1) & 3];
      const float a = rbf(((i & 1) ? bhi(wd) : blo(wd)) - mb);
      const float q0 = a * r, qq = fmaf(fmaf(-q0, den, a), r, q0);
      xn2[i] = cvtpk(qq, qq);
      PIN(xn2[i]);
    }
  }
  SB();
  GAS u64* myslot = (GAS u64*)(ws + WS_GRAN) + ((size_t)(b * 32 + (tid >> 4)) * 32 + h) * 16 + (tid & 15);
  __hip_atomic_store(myslot, (u64)epoch << 32, RLX_AGENT);
  ka[6] = ldk(0, 6); wr[4] = ldq(4); ka[7] = ldk(0, 7); wr[5] = ldq(5); va[0] = ldv(0, 0); SB();
  va[1] = ldv(0, 1); wr[6] = ldq(6); va[2] = ldv(0, 2); wr[7] = ldq(7); va[3] = ldv(0, 3); SB();
  va[4] = ldv(0, 4); wr[8] = ldq(8); va[5] = ldv(0, 5); wr[9] = ldq(9); va[6] = ldv(0, 6); SB();
  va[7] = ldv(0, 7); wr[10] = ldq(10); kb[0] = ldk(1, 0); wr[11] = ldq(11); kb[1] = ldk(1, 1); SB();
  kb[2] = ldk(1, 2); wr[12] = ldq(12); kb[3] = ldk(1, 3); wr[13] = ldq(13); kb[4] = ldk(1, 4); SB();
  kb[5] = ldk(1, 5); wr[14] = ldq(14); kb[6] = ldk(1, 6); wr[15] = ldq(15); SB();
  __builtin_amdgcn_s_barrier();
  kb[7] = ldk(1, 7);
#pragma unroll
  for (int t = 0; t < T; t++) vb[t] = ldv(1, t);
  SB();
  {
    f32x4 acc = {0.f, 0.f, 0.f, 0.f};
#pragma unroll
    for (int i = 0; i < 16; i++) acc = mfma(m4 & xn2[i], wr[i], acc);
    if (lane < 32) *(f32x4*)&lds.qpart[w][8 * c + 4 * g] = acc;
  }
  u32 xn1[8];
#pragma unroll
  for (int i = 0; i < 8; i++) { const u32 e0 = xn2[i], e1 = xn2[8 + i]; xn1[i] = c < 8 ? e0 : e1; PIN(xn1[i]); }
  lds_barrier();
  u32x4 qp;
  {
    f32x4 a = *(const f32x4*)&lds.qpart[0][8 * c], bq = *(const f32x4*)&lds.qpart[0][8 * c + 4];
#pragma unroll
    for (int i = 1; i < NW; i++) { a += *(const f32x4*)&lds.qpart[i][8 * c]; bq += *(const f32x4*)&lds.qpart[i][8 * c + 4]; }
    qp[0] = cvtpk(a[0], a[1]); qp[1] = cvtpk(a[2], a[3]); qp[2] = cvtpk(bq[0], bq[1]); qp[3] = cvtpk(bq[2], bq[3]);
  }

  const rsrc_t r1 = mkrsrc(w1 + hm * 64, (DM * FF - hm * 64) * 2);
  const rsrc_t r2 = mkrsrc(w2 + (size_t)hm * 64 * DM, 64 * DM * 2);
  const rsrc_t ro = mkrsrc(wo + (size_t)h * DH * DM, DH * DM * 2);
  const u32 off1 = (lane >> 3) * (8 * FF * 2) + (lane & 7) * 16, off2 = g * (DM * 2) + c * 16;
  auto side = [&](auto E) {
    constexpr int e = decltype(E)::v;
    if constexpr (e < 0) {
    } else if constexpr (e < 8) wr[e] = bld<WAUX>(r1, off1, (64 * w + e) * (FF * 2));
    else if constexpr (e < 16) wr[e] = bld<WAUX>(r2, off2 + ((e - 8) >> 1) * 256, (8 * w + 4 * (e & 1)) * (DM * 2));
    else if constexpr (e < 24) kb[e - 16] = bld<WAUX>(ro, off2 + ((e - 16) >> 2) * 256, (16 * w + 4 * (e & 3)) * (DM * 2));
    else vb[e - 24] = bld<WAUX>(ro, off2 + ((e - 16) >> 2) * 256, (16 * w + 4 * (e & 3)) * (DM * 2));
  };

  float m = -INFINITY, l = 0.f;
  f32x4 oacc = {0.f, 0.f, 0.f, 0.f};
  f32x4 y[4];
  constexpr float SCALE = 0.08838834764831845f, L2E = 1.4426950408889634f;

  auto tile = [&](u32x4 (&k)[T], u32x4 (&v)[T], int nx, auto REFILL, auto E0) {
    constexpr bool refill = decltype(REFILL)::v;
    constexpr int e0 = decltype(E0)::v;
    float s[T];
#pragma unroll
    for (int t = 0; t < T; t++) {
      float d = dot2(k[t][0], qp[0], 0.f);
      d = dot2(k[t][1], qp[1], d); d = dot2(k[t][2], qp[2], d); d = dot2(k[t][3], qp[3], d);
      PIN(d);
      s[t] = d;
      if constexpr (refill) k[t] = ldk(nx, t);
      if (t == 3) side(IC<e0>{});
      if (t == 7) side(IC<(e0 < 0 ? -1 : e0 + 1)>{});
      SB();
    }
    float tm = -INFINITY;
#pragma unroll
    for (int t = 0; t < T; t++) { s[t] = rbf(rbf(rowsum16(s[t])) * SCALE) * L2E; tm = fmaxf(tm, s[t]); }
    tm = xrow_max(tm);
    const float mn = fmaxf(m, tm), alpha = __builtin_amdgcn_exp2f(m - mn);
    m = mn;
    float ps = 0.f;
#pragma unroll
    for (int t = 0; t < T; t++) { s[t] = __builtin_amdgcn_exp2f(s[t] - mn); ps += s[t]; }
    l = fmaf(l, alpha, ps);
    oacc *= alpha;
    SB();
#pragma unroll
    for (int t = 0; t < T; t++) {
      oacc = mfma(m4 & cvtpk(s[t], s[t]), v[t], oacc);
      PIN(oacc);
      if constexpr (refill) v[t] = ldv(nx, t);
      if (t == 3) side(IC<(e0 < 0 ? -1 : e0 + 2)>{});
      if (t == 7) side(IC<(e0 < 0 ? -1 : e0 + 3)>{});
      SB();
    }
  };

  tile(ka, va, 2, IC<1>{}, IC<0>{});
  tile(kb, vb, 0, IC<0>{}, IC<-1>{});
  tile(ka, va, 3, IC<1>{}, IC<4>{});
  tile(ka, va, 4, IC<1>{}, IC<8>{});
  const float b1v = asf((u32)b1[64 * hm + 8 * w + 4 * (c & 1) + g] << 16);
  tile(ka, va, 5, IC<1>{}, IC<12>{});
  {
    f32x4 acc = {0.f, 0.f, 0.f, 0.f};
#pragma unroll
    for (int i = 0; i < 8; i++) acc = mfma(m8 & xn1[i], wr[i], acc);
    if ((g >> 1) == (c >> 3)) *(f32x4*)&lds.hpart[2 * w + (g >> 1)][8 * (c & 7) + 4 * (g & 1)] = acc;
  }
  tile(ka, va, 6, IC<1>{}, IC<16>{});
  {
    lds_barrier();
    const int u = 8 * w + 4 * (c & 1) + g;
    float hs = lds.hpart[2 * (c >> 1)][u] + lds.hpart[2 * (c >> 1) + 1][u];
    hs += dpp<XOR2>(hs); hs += dpp<ROR4>(hs); hs += dpp<ROR8>(hs);
    const float hv = rbf(rbf(hs) + b1v);
    const float e = __builtin_amdgcn_exp2f((2.f * L2E * 0.7978845608028654f) * (hv + 0.044715f * hv * hv * hv));
    const float th = 1.f - 2.f / (e + 1.f);
    float gl = 0.5f * hv * (1.f + th);
    gl = has_mlp ? gl : 0.f;
    const u32 g2 = cvtpk(gl, gl);
    const u32 ga = dppu<0x00>(g2), gb = dppu<0x55>(g2);
#pragma unroll
    for (int cb = 0; cb < 4; cb++) {
      f32x4 a = {0.f, 0.f, 0.f, 0.f};
      a = mfma(m4 & ga, wr[8 + 2 * cb], a);
      y[cb] = mfma(m4 & gb, wr[9 + 2 * cb], a);
      PIN(y[cb]);
    }
  }
  tile(ka, va, 7, IC<1>{}, IC<20>{});
  tile(ka, va, 8, IC<1>{}, IC<24>{});
  tile(ka, va, 9, IC<1>{}, IC<28>{});
#pragma unroll 1
  for (int it = 10; it < NIT; it++) {
    tile(ka, va, it, IC<1>{}, IC<-1>{});
  }
  tile(ka, va, 0, IC<0>{}, IC<-1>{});

  l = xrow_sum(l);
  if (lane < 32) *(f32x4*)&lds.orec[w][8 * c + 4 * g] = oacc;
  if (lane == 0) { lds.ml[0][w] = m; lds.ml[1][w] = l; }
  lds_barrier();
  {
    const f32x4 ma = *(const f32x4*)&lds.ml[0][0], mb = *(const f32x4*)&lds.ml[0][4];
    const f32x4 la = *(const f32x4*)&lds.ml[1][0], lb = *(const f32x4*)&lds.ml[1][4];
    const int d = 16 * w + 4 * (c & 3) + g, r0 = 2 * (c >> 2);
    const float o0 = lds.orec[r0][d], o1 = lds.orec[r0 + 1][d];
    const float m0 = lds.ml[0][r0], m1 = lds.ml[0][r0 + 1];
    const float M = fmaxf(fmaxf(fmaxf(ma[0], ma[1]), fmaxf(ma[2], ma[3])), fmaxf(fmaxf(mb[0], mb[1]), fmaxf(mb[2], mb[3])));
    float L = 0.f;
#pragma unroll
    for (int i = 0; i < 4; i++) { L = fmaf(__builtin_amdgcn_exp2f(ma[i] - M), la[i], L); L = fmaf(__builtin_amdgcn_exp2f(mb[i] - M), lb[i], L); }
    float o = __builtin_amdgcn_exp2f(m0 - M) * o0 + __builtin_amdgcn_exp2f(m1 - M) * o1;
    o += dpp<ROR4>(o); o += dpp<ROR8>(o);
    o = o / L;
    const u32 o2 = cvtpk(o, o);
    u32 oq[4];
    oq[0] = dppu<0x00>(o2); oq[1] = dppu<0x55>(o2); oq[2] = dppu<0xAA>(o2); oq[3] = dppu<0xFF>(o2);
#pragma unroll
    for (int t = 0; t < 8; t++) y[t >> 2] = mfma(m4 & oq[t & 3], kb[t], y[t >> 2]);
#pragma unroll
    for (int t = 0; t < 8; t++) y[2 + (t >> 2)] = mfma(m4 & oq[t & 3], vb[t], y[2 + (t >> 2)]);
    if (lane < 32) {
#pragma unroll
      for (int cb = 0; cb < 4; cb++) *(f32x4*)&lds.ypart[w][128 * cb + 8 * c + 4 * g] = y[cb];
    }
  }
  lds_barrier();
  {
    float s = lds.ypart[0][tid];
#pragma unroll
    for (int i = 1; i < NW; i++) s += lds.ypart[i][tid];
    __hip_atomic_store(myslot, ((u64)(epoch + 1u) << 32) | asu(s), RLX_AGENT);
  }
  if (w == 0) {
    const float base = asf((u32)x[b * DM + 16 * h + c] << 16) + asf((u32)b2[16 * h + c] << 16);
    GAS u64* src = (GAS u64*)(ws + WS_GRAN) + ((size_t)(b * 32 + h) * 32 + g) * 16 + c;
    u64 gv[8];
    for (u32 spins = 0;; spins++) {
      bool ok = true;
#pragma unroll
      for (int i = 0; i < 8; i++) { gv[i] = __hip_atomic_load(src + i * 64, RLX_AGENT); ok &= (u32)(gv[i] >> 32) == epoch + 1u; }
      if (__all(ok) || spins > (1u << 20)) break;
      __builtin_amdgcn_s_sleep(8);
    }
    float s = 0.f;
#pragma unroll
    for (int i = 0; i < 8; i++) s += asf((u32)gv[i]);
    s = xrow_sum(s) + base;
    if (lane < 16) out[b * DM + 16 * h + c] = (u16)(cvtpk(s, s) & 0xffffu);
    if (h == 0 && lane == 0) __hip_atomic_store(ep, epoch + 1u, RLX_AGENT);
  }
}

extern "C" void kernel_launch(void* const* d_in, const int* in_sizes, int n_in, void* d_out, int out_size, void* d_ws,
                              size_t ws_size, hipStream_t stream) {
  for (int i = 0; i < n_in; i++) { void* base = 0; size_t sz = 0; hipMemGetAddressRange((hipDeviceptr_t*)&base, &sz, d_in[i]); hipPointerAttribute_t at; hipPointerGetAttributes(&at, d_in[i]);
    fprintf(stderr, "in%d %p n=%d base %p sz %zu type %d flags %u\n", i, d_in[i], in_sizes[i], base, sz, (int)at.type, at.allocationFlags); }
  { void* base = 0; size_t sz = 0; hipMemGetAddressRange((hipDeviceptr_t*)&base, &sz, d_ws); fprintf(stderr, "ws %p base %p sz %zu out %p\n", d_ws, base, sz, d_out); }
  fused_decode<<<NB * NH, NW * 64, 0, stream>>>((const u16*)d_in[0], (const u16*)d_in[1], (const u16*)d_in[2], (const u16*)d_in[3],
                                                (const u16*)d_in[4], (const u16*)d_in[6], (const u16*)d_in[7], (const u16*)d_in[8],
                                                (const u16*)d_in[9], (u16*)d_out, (unsigned char*)d_ws);
}
```

```cpp
#include <hip/hip_runtime.h>
#include <cstdint>

typedef unsigned short u16;
typedef unsigned u32;
typedef unsigned long long u64;
typedef u32 u32x4 __attribute__((ext_vector_type(4)));
typedef float f32x4 __attribute__((ext_vector_type(4)));
typedef short bf16x8 __attribute__((ext_vector_type(8)));
typedef __bf16 bf16x2 __attribute__((ext_vector_type(2)));
typedef __amdgpu_buffer_rsrc_t rsrc_t;
#define GAS __attribute__((address_space(1)))
#define RLX_AGENT __ATOMIC_RELAXED, __HIP_MEMORY_SCOPE_AGENT

#define LOOPBAR 0
#define WAUX 0
#define KAUX 19
#define VAUX 19
#define STAMPS 0

constexpr int NB = 8, DM = 512, NH = 32, SK = 4096, DH = 128, FF = 1792;
constexpr int NW = 8, T = 8, NIT = SK / (4 * T * NW);
constexpr int TILE_STRIDE = NW * T * 1024;
constexpr int MLPH = FF / 64;
constexpr size_t WS_GRAN = 65536;
constexpr size_t WS_STAMP = 4u << 20;

__device__ __forceinline__ float asf(u32 v) { return __builtin_bit_cast(float, v); }
__device__ __forceinline__ u32 asu(float v) { return __builtin_bit_cast(u32, v); }
__device__ __forceinline__ float blo(u32 v) { return asf(v << 16); }
__device__ __forceinline__ float bhi(u32 v) { return asf(v & 0xffff0000u); }
__device__ __forceinline__ u32 cvtpk(float lo, float hi) {
  bf16x2 r = {(__bf16)lo, (__bf16)hi};
  return __builtin_bit_cast(u32, r);
}
__device__ __forceinline__ float rbf(float x) { return (float)(__bf16)x; }
__device__ __forceinline__ float dot2(u32 a, u32 b, float c) {
  return __builtin_amdgcn_fdot2_f32_bf16(__builtin_bit_cast(bf16x2, a), __builtin_bit_cast(bf16x2, b), c, false);
}
template <int CTRL> __device__ __forceinline__ float dpp(float x) {
  return asf(__builtin_amdgcn_mov_dpp(asu(x), CTRL, 0xf, 0xf, true));
}
template <int CTRL> __device__ __forceinline__ u32 dppu(u32 x) { return __builtin_amdgcn_mov_dpp(x, CTRL, 0xf, 0xf, true); }
constexpr int XOR1 = 0xB1, XOR2 = 0x4E, HMIR = 0x141, ROR8 = 0x128, ROR4 = 0x124;
__device__ __forceinline__ float rowsum16(float x) {
  x += dpp<XOR1>(x); x += dpp<XOR2>(x); x += dpp<HMIR>(x); x += dpp<ROR8>(x); return x;
}
__device__ __forceinline__ float xrow_max(float x) {
  auto s = __builtin_amdgcn_permlane16_swap(asu(x), asu(x), false, false);
  x = fmaxf(asf(s[0]), asf(s[1]));
  auto t = __builtin_amdgcn_permlane32_swap(asu(x), asu(x), false, false);
  return fmaxf(asf(t[0]), asf(t[1]));
}
__device__ __forceinline__ float xrow_sum(float x) {
  auto s = __builtin_amdgcn_permlane16_swap(asu(x), asu(x), false, false);
  x = asf(s[0]) + asf(s[1]);
  auto t = __builtin_amdgcn_permlane32_swap(asu(x), asu(x), false, false);
  return asf(t[0]) + asf(t[1]);
}
__device__ __forceinline__ f32x4 mfma(u32x4 a, u32x4 b, f32x4 c) {
  return __builtin_amdgcn_mfma_f32_16x16x32_bf16(__builtin_bit_cast(bf16x8, a), __builtin_bit_cast(bf16x8, b), c, 0, 0, 0);
}
__device__ __forceinline__ rsrc_t mkrsrc(const void* p, u32 bytes) {
  return __builtin_amdgcn_make_buffer_rsrc((void*)p, 0, bytes, 0x00020000);
}
template <int AUX> __device__ __forceinline__ u32x4 bld(rsrc_t r, u32 voff, u32 soff) {
  return __builtin_amdgcn_raw_buffer_load_b128(r, voff, soff, AUX);
}
#define SB() __builtin_amdgcn_sched_barrier(0)
#define PIN(x) asm volatile("" : "+v"(x))
__device__ __forceinline__ void lds_barrier() {
  asm volatile("s_waitcnt lgkmcnt(0)\n\ts_barrier" ::: "memory");
}
template <int N> struct IC { static constexpr int v = N; };

struct Lds {
  float qpart[NW][DH];
  float hpart[2 * NW][64];
  float orec[NW][DH];
  float ml[2][NW];
  float ypart[NW][DM];
};

__global__ __launch_bounds__(NW * 64, 2) void fused_decode(
    const u16* __restrict__ x, const u16* __restrict__ Kc, const u16* __restrict__ Vc, const u16* __restrict__ wq,
    const u16* __restrict__ wo, const u16* __restrict__ w1, const u16* __restrict__ b1, const u16* __restrict__ w2,
    const u16* __restrict__ b2, u16* __restrict__ out, unsigned char* __restrict__ ws) {
  __shared__ __attribute__((aligned(16))) Lds lds;
  const int tid = threadIdx.x, lane = tid & 63, w = __builtin_amdgcn_readfirstlane(tid >> 6);
  const int g = lane >> 4, c = lane & 15;
  const int bid = blockIdx.x, h = (bid & 7) * 4 + ((bid >> 3) & 3), b = bid >> 5;
  const bool has_mlp = h < MLPH;
  const int hm = has_mlp ? h : 0;

  const size_t bh = (size_t)(b * NH + h) * (SK * DH);
  const rsrc_t rk = mkrsrc(Kc + bh, SK * DH * 2), rv = mkrsrc(Vc + bh, SK * DH * 2);
  const rsrc_t rx = mkrsrc(x + b * DM, DM * 2);
  const rsrc_t rq = mkrsrc(wq + h * DH, (DM * NH * DH - h * DH) * 2);
  const u32 l16 = lane * 16, l16b = l16 + 4096;
  const u32 kw = w * (T * 1024);
  auto ldk = [&](int (tile), int t) { return bld<KAUX>(rk, (t < 4 ? l16 : l16b) + (t & 3) * 1024, kw + tile * TILE_STRIDE); };
  auto ldv = [&](int (tile), int t) { return bld<VAUX>(rv, (t < 4 ? l16 : l16b) + (t & 3) * 1024, kw + tile * TILE_STRIDE); };
  const u32 qoff = g * (16 * NH * DH * 2) + c * 16;
  auto ldq = [&](int i) { return bld<WAUX>(rq, qoff, (64 * w + i) * (NH * DH * 2)); };

  u32x4 ka[T], va[T], kb[T], vb[T], wr[16];
  ka[0] = ldk(0, 0); ka[1] = ldk(0, 1);
  const u32x4 x8 = bld<0>(rx, l16, 0);
  const u32x4 xa = bld<0>(rx, g * 32, w * 128), xb = bld<0>(rx, g * 32 + 16, w * 128);
  GAS u32* ep = (GAS u32*)(ws + 256 * b);
  const u32 epoch = __hip_atomic_load(ep, RLX_AGENT);
  SB();
  wr[0] = ldq(0); ka[2] = ldk(0, 2); wr[1] = ldq(1); ka[3] = ldk(0, 3); SB();
  wr[2] = ldq(2); ka[4] = ldk(0, 4); wr[3] = ldq(3); ka[5] = ldk(0, 5); SB();

  u32x4 m4, m8;
#pragma unroll
  for (int r = 0; r < 4; r++) {
    const u32 mk = (((c & 7) >> 1) == r) ? ((c & 1) ? 0xffff0000u : 0x0000ffffu) : 0u;
    m8[r] = mk; m4[r] = c < 8 ? mk : 0u;
  }
  u32 xn2[16];
  {
    float s = 0.f;
#pragma unroll
    for (int j = 0; j < 4; j++) s += blo(x8[j]) + bhi(x8[j]);
    s = xrow_sum(rowsum16(s));
    const float mean = s * (1.f / DM);
    float q = 0.f;
#pragma unroll
    for (int j = 0; j < 4; j++) { const float d0 = blo(x8[j]) - mean, d1 = bhi(x8[j]) - mean; q = fmaf(d0, d0, q); q = fmaf(d1, d1, q); }
    q = xrow_sum(rowsum16(q));
    const float mb = rbf(mean), den = rbf(sqrtf(rbf(rbf(q * (1.f / DM)) + 1e-5f)));
    const float r = 1.f / den;
#pragma unroll
    for (int i = 0; i < 16; i++) {
      const u32 wd = i < 8 ? (u32)xa[(i >> 1) & 3] : (u32)xb[(i >> 1) & 3];
      const float a = rbf(((i & 1) ? bhi(wd) : blo(wd)) - mb);
      const float q0 = a * r, qq = fmaf(fmaf(-q0, den, a), r, q0);
      xn2[i] = cvtpk(qq, qq);
      PIN(xn2[i]);
    }
  }
  SB();
  GAS u64* myslot = (GAS u64*)(ws + WS_GRAN) + ((size_t)(b * 32 + (tid >> 4)) * 32 + h) * 16 + (tid & 15);
  __hip_atomic_store(myslot, (u64)epoch << 32, RLX_AGENT);
  ka[6] = ldk(0, 6); wr[4] = ldq(4); ka[7] = ldk(0, 7); wr[5] = ldq(5); va[0] = ldv(0, 0); SB();
  va[1] = ldv(0, 1); wr[6] = ldq(6); va[2] = ldv(0, 2); wr[7] = ldq(7); va[3] = ldv(0, 3); SB();
  va[4] = ldv(0, 4); wr[8] = ldq(8); va[5] = ldv(0, 5); wr[9] = ldq(9); va[6] = ldv(0, 6); SB();
  va[7] = ldv(0, 7); wr[10] = ldq(10); kb[0] = ldk(1, 0); wr[11] = ldq(11); kb[1] = ldk(1, 1); SB();
  kb[2] = ldk(1, 2); wr[12] = ldq(12); kb[3] = ldk(1, 3); wr[13] = ldq(13); kb[4] = ldk(1, 4); SB();
  kb[5] = ldk(1, 5); wr[14] = ldq(14); kb[6] = ldk(1, 6); wr[15] = ldq(15); SB();
  __builtin_amdgcn_s_barrier();
  kb[7] = ldk(1, 7);
#pragma unroll
  for (int t = 0; t < T; t++) vb[t] = ldv(1, t);
  SB();
  {
    f32x4 acc = {0.f, 0.f, 0.f, 0.f};
#pragma unroll
    for (int i = 0; i < 16; i++) acc = mfma(m4 & xn2[i], wr[i], acc);
    if (lane < 32) *(f32x4*)&lds.qpart[w][8 * c + 4 * g] = acc;
  }
  u32 xn1[8];
#pragma unroll
  for (int i = 0; i < 8; i++) { const u32 e0 = xn2[i], e1 = xn2[8 + i]; xn1[i] = c < 8 ? e0 : e1; PIN(xn1[i]); }
  lds_barrier();
  u32x4 qp;
  {
    f32x4 a = *(const f32x4*)&lds.qpart[0][8 * c], bq = *(const f32x4*)&lds.qpart[0][8 * c + 4];
#pragma unroll
    for (int i = 1; i < NW; i++) { a += *(const f32x4*)&lds.qpart[i][8 * c]; bq += *(const f32x4*)&lds.qpart[i][8 * c + 4]; }
    qp[0] = cvtpk(a[0], a[1]); qp[1] = cvtpk(a[2], a[3]); qp[2] = cvtpk(bq[0], bq[1]); qp[3] = cvtpk(bq[2], bq[3]);
  }

  const rsrc_t r1 = mkrsrc(w1 + hm * 64, (DM * FF - hm * 64) * 2);
  const rsrc_t r2 = mkrsrc(w2 + (size_t)hm * 64 * DM, 64 * DM * 2);
  const rsrc_t ro = mkrsrc(wo + (size_t)h * DH * DM, DH * DM * 2);
  const u32 off1 = (lane >> 3) * (8 * FF * 2) + (lane & 7) * 16, off2 = g * (DM * 2) + c * 16;
  auto side = [&](auto E) {
    constexpr int e = decltype(E)::v;
    if constexpr (e < 0) {
    } else if constexpr (e < 8) wr[e] = bld<WAUX>(r1, off1, (64 * w + e) * (FF * 2));
    else if constexpr (e < 16) wr[e] = bld<WAUX>(r2, off2 + ((e - 8) >> 1) * 256, (8 * w + 4 * (e & 1)) * (DM * 2));
    else if constexpr (e < 24) kb[e - 16] = bld<WAUX>(ro, off2 + ((e - 16) >> 2) * 256, (16 * w + 4 * (e & 3)) * (DM * 2));
    else vb[e - 24] = bld<WAUX>(ro, off2 + ((e - 16) >> 2) * 256, (16 * w + 4 * (e & 3)) * (DM * 2));
  };

  float m = -INFINITY, l = 0.f;
  f32x4 oacc = {0.f, 0.f, 0.f, 0.f};
  f32x4 y[4];
  constexpr float SCALE = 0.08838834764831845f, L2E = 1.4426950408889634f;

  auto tile = [&](u32x4 (&k)[T], u32x4 (&v)[T], int nx, auto REFILL, auto E0) {
    constexpr bool refill = decltype(REFILL)::v;
    constexpr int e0 = decltype(E0)::v;
    float s[T];
#pragma unroll
    for (int t = 0; t < T; t++) {
      float d = dot2(k[t][0], qp[0], 0.f);
      d = dot2(k[t][1], qp[1], d); d = dot2(k[t][2], qp[2], d); d = dot2(k[t][3], qp[3], d);
      PIN(d);
      s[t] = d;
      if constexpr (refill) k[t] = ldk(nx, t);
      if (t == 3) side(IC<e0>{});
      if (t == 7) side(IC<(e0 < 0 ? -1 : e0 + 1)>{});
      SB();
    }
    float tm = -INFINITY;
#pragma unroll
    for (int t = 0; t < T; t++) { s[t] = rbf(rbf(rowsum16(s[t])) * SCALE) * L2E; tm = fmaxf(tm, s[t]); }
    tm = xrow_max(tm);
    const float mn = fmaxf(m, tm), alpha = __builtin_amdgcn_exp2f(m - mn);
    m = mn;
    float ps = 0.f;
#pragma unroll
    for (int t = 0; t < T; t++) { s[t] = __builtin_amdgcn_exp2f(s[t] - mn); ps += s[t]; }
    l = fmaf(l, alpha, ps);
    oacc *= alpha;
    SB();
#pragma unroll
    for (int t = 0; t < T; t++) {
      oacc = mfma(m4 & cvtpk(s[t], s[t]), v[t], oacc);
      PIN(oacc);
      if constexpr (refill) v[t] = ldv(nx, t);
      if (t == 3) side(IC<(e0 < 0 ? -1 : e0 + 2)>{});
      if (t == 7) side(IC<(e0 < 0 ? -1 : e0 + 3)>{});
      SB();
    }
  };

  tile(ka, va, 2, IC<1>{}, IC<0>{});
  tile(kb, vb, 0, IC<0>{}, IC<-1>{});
  tile(ka, va, 3, IC<1>{}, IC<4>{});
  tile(ka, va, 4, IC<1>{}, IC<8>{});
  const float b1v = asf((u32)b1[64 * hm + 8 * w + 4 * (c & 1) + g] << 16);
  tile(ka, va, 5, IC<1>{}, IC<12>{});
  {
    f32x4 acc = {0.f, 0.f, 0.f, 0.f};
#pragma unroll
    for (int i = 0; i < 8; i++) acc = mfma(m8 & xn1[i], wr[i], acc);
    if ((g >> 1) == (c >> 3)) *(f32x4*)&lds.hpart[2 * w + (g >> 1)][8 * (c & 7) + 4 * (g & 1)] = acc;
  }
  tile(ka, va, 6, IC<1>{}, IC<16>{});
  {
    lds_barrier();
    const int u = 8 * w + 4 * (c & 1) + g;
    float hs = lds.hpart[2 * (c >> 1)][u] + lds.hpart[2 * (c >> 1) + 1][u];
    hs += dpp<XOR2>(hs); hs += dpp<ROR4>(hs); hs += dpp<ROR8>(hs);
    const float hv = rbf(rbf(hs) + b1v);
    const float e = __builtin_amdgcn_exp2f((2.f * L2E * 0.7978845608028654f) * (hv + 0.044715f * hv * hv * hv));
    const float th = 1.f - 2.f / (e + 1.f);
    float gl = 0.5f * hv * (1.f + th);
    gl = has_mlp ? gl : 0.f;
    const u32 g2 = cvtpk(gl, gl);
    const u32 ga = dppu<0x00>(g2), gb = dppu<0x55>(g2);
#pragma unroll
    for (int cb = 0; cb < 4; cb++) {
      f32x4 a = {0.f, 0.f, 0.f, 0.f};
      a = mfma(m4 & ga, wr[8 + 2 * cb], a);
      y[cb] = mfma(m4 & gb, wr[9 + 2 * cb], a);
      PIN(y[cb]);
    }
  }
  tile(ka, va, 7, IC<1>{}, IC<20>{});
  tile(ka, va, 8, IC<1>{}, IC<24>{});
  tile(ka, va, 9, IC<1>{}, IC<28>{});
#pragma unroll 1
  for (int it = 10; it < NIT; it++) {
    tile(ka, va, it, IC<1>{}, IC<-1>{});
  }
  tile(ka, va, 0, IC<0>{}, IC<-1>{});

  l = xrow_sum(l);
  if (lane < 32) *(f32x4*)&lds.orec[w][8 * c + 4 * g] = oacc;
  if (lane == 0) { lds.ml[0][w] = m; lds.ml[1][w] = l; }
  lds_barrier();
  {
    const f32x4 ma = *(const f32x4*)&lds.ml[0][0], mb = *(const f32x4*)&lds.ml[0][4];
    const f32x4 la = *(const f32x4*)&lds.ml[1][0], lb = *(const f32x4*)&lds.ml[1][4];
    const int d = 16 * w + 4 * (c & 3) + g, r0 = 2 * (c >> 2);
    const float o0 = lds.orec[r0][d], o1 = lds.orec[r0 + 1][d];
    const float m0 = lds.ml[0][r0], m1 = lds.ml[0][r0 + 1];
    const float M = fmaxf(fmaxf(fmaxf(ma[0], ma[1]), fmaxf(ma[2], ma[3])), fmaxf(fmaxf(mb[0], mb[1]), fmaxf(mb[2], mb[3])));
    float L = 0.f;
#pragma unroll
    for (int i = 0; i < 4; i++) { L = fmaf(__builtin_amdgcn_exp2f(ma[i] - M), la[i], L); L = fmaf(__builtin_amdgcn_exp2f(mb[i] - M), lb[i], L); }
    float o = __builtin_amdgcn_exp2f(m0 - M) * o0 + __builtin_amdgcn_exp2f(m1 - M) * o1;
    o += dpp<ROR4>(o); o += dpp<ROR8>(o);
    o = o / L;
    const u32 o2 = cvtpk(o, o);
    u32 oq[4];
    oq[0] = dppu<0x00>(o2); oq[1] = dppu<0x55>(o2); oq[2] = dppu<0xAA>(o2); oq[3] = dppu<0xFF>(o2);
#pragma unroll
    for (int t = 0; t < 8; t++) y[t >> 2] = mfma(m4 & oq[t & 3], kb[t], y[t >> 2]);
#pragma unroll
    for (int t = 0; t < 8; t++) y[2 + (t >> 2)] = mfma(m4 & oq[t & 3], vb[t], y[2 + (t >> 2)]);
    if (lane < 32) {
#pragma unroll
      for (int cb = 0; cb < 4; cb++) *(f32x4*)&lds.ypart[w][128 * cb + 8 * c + 4 * g] = y[cb];
    }
  }
  lds_barrier();
  {
    float s = lds.ypart[0][tid];
#pragma unroll
    for (int i = 1; i < NW; i++) s += lds.ypart[i][tid];
    __hip_atomic_store(myslot, ((u64)(epoch + 1u) << 32) | asu(s), RLX_AGENT);
  }
  if (w == 0) {
    const float base = asf((u32)x[b * DM + 16 * h + c] << 16) + asf((u32)b2[16 * h + c] << 16);
    GAS u64* src = (GAS u64*)(ws + WS_GRAN) + ((size_t)(b * 32 + h) * 32 + g) * 16 + c;
    u64 gv[8];
    for (u32 spins = 0;; spins++) {
      bool ok = true;
#pragma unroll
      for (int i = 0; i < 8; i++) { gv[i] = __hip_atomic_load(src + i * 64, RLX_AGENT); ok &= (u32)(gv[i] >> 32) == epoch + 1u; }
      if (__all(ok) || spins > (1u << 20)) break;
      __builtin_amdgcn_s_sleep(8);
    }
    float s = 0.f;
#pragma unroll
    for (int i = 0; i < 8; i++) s += asf((u32)gv[i]);
    s = xrow_sum(s) + base;
    if (lane < 16) out[b * DM + 16 * h + c] = (u16)(cvtpk(s, s) & 0xffffu);
    if (h == 0 && lane == 0) __hip_atomic_store(ep, epoch + 1u, RLX_AGENT);
  }
}

extern "C" void kernel_launch(void* const* d_in, const int* in_sizes, int n_in, void* d_out, int out_size, void* d_ws,
                              size_t ws_size, hipStream_t stream) {
  fused_decode<<<NB * NH, NW * 64, 0, stream>>>((const u16*)d_in[0], (const u16*)d_in[1], (const u16*)d_in[2], (const u16*)d_in[3],
                                                (const u16*)d_in[4], (const u16*)d_in[6], (const u16*)d_in[7], (const u16*)d_in[8],
                                                (const u16*)d_in[9], (u16*)d_out, (unsigned char*)d_ws);
}
```

```cpp
#include <hip/hip_runtime.h>
#include <cstdint>

typedef unsigned short u16;
typedef unsigned u32;
typedef unsigned long long u64;
typedef u32 u32x4 __attribute__((ext_vector_type(4)));
typedef float f32x4 __attribute__((ext_vector_type(4)));
typedef short bf16x8 __attribute__((ext_vector_type(8)));
typedef __bf16 bf16x2 __attribute__((ext_vector_type(2)));
typedef __amdgpu_buffer_rsrc_t rsrc_t;
#define GAS __attribute__((address_space(1)))
#define LAS __attribute__((address_space(3)))
#define RLX_AGENT __ATOMIC_RELAXED, __HIP_MEMORY_SCOPE_AGENT

#define WIN 22
#define GRP 2
#define WAUX 0
#define NOSIDE 0
#define HSLEEP 40
#define SN1 144
#define SN2 528
#define SN3 V0
#define STAMPS 0

constexpr int NB = 8, DM = 512, NH = 32, SK = 4096, DH = 128, FF = 1792;
constexpr int NC = 4, NHOLD = 8, NW = 1 + NC + NHOLD;
constexpr int MLPH = FF / 64;
constexpr int RING = 128;
constexpr int K0 = 16, V0 = K0 + 1024, END = V0 + 1024;
constexpr size_t WS_Y = 65536, WS_Q = 2u << 20, WS_STAMP = 8u << 20;
constexpr u32 SPIN_CAP = 1u << 18;
constexpr int L_RING = 0, L_SC = RING * 1024, L_HP = L_SC + SK * 4, L_OP = L_HP + 2 * NHOLD * 64 * 4, L_Q = L_OP + NC * DH * 4;
constexpr int L_FULL = L_Q + 256, L_NEXT = L_FULL + 256, L_QRDY = L_NEXT + 16, L_ML = L_QRDY + 16, L_MF = L_ML + 32, L_HF = L_MF + 16, L_END = L_HF + 32;
constexpr int L_YP = 0;

__device__ __forceinline__ float asf(u32 v) { return __builtin_bit_cast(float, v); }
__device__ __forceinline__ u32 asu(float v) { return __builtin_bit_cast(u32, v); }
__device__ __forceinline__ float blo(u32 v) { return asf(v << 16); }
__device__ __forceinline__ float bhi(u32 v) { return asf(v & 0xffff0000u); }
__device__ __forceinline__ u32 cvtpk(float lo, float hi) {
  bf16x2 r = {(__bf16)lo, (__bf16)hi};
  return __builtin_bit_cast(u32, r);
}
__device__ __forceinline__ float rbf(float x) { return (float)(__bf16)x; }
__device__ __forceinline__ float dot2(u32 a, u32 b, float c) {
  return __builtin_amdgcn_fdot2_f32_bf16(__builtin_bit_cast(bf16x2, a), __builtin_bit_cast(bf16x2, b), c, false);
}
template <int CTRL> __device__ __forceinline__ float dpp(float x) {
  return asf(__builtin_amdgcn_mov_dpp(asu(x), CTRL, 0xf, 0xf, true));
}
template <int CTRL> __device__ __forceinline__ u32 dppu(u32 x) { return __builtin_amdgcn_mov_dpp(x, CTRL, 0xf, 0xf, true); }
constexpr int XOR1 = 0xB1, XOR2 = 0x4E, HMIR = 0x141, ROR8 = 0x128, ROR4 = 0x124;
__device__ __forceinline__ float xrow_max(float x) {
  auto s = __builtin_amdgcn_permlane16_swap(asu(x), asu(x), false, false);
  x = fmaxf(asf(s[0]), asf(s[1]));
  auto t = __builtin_amdgcn_permlane32_swap(asu(x), asu(x), false, false);
  return fmaxf(asf(t[0]), asf(t[1]));
}
__device__ __forceinline__ float xrow_sum(float x) {
  auto s = __builtin_amdgcn_permlane16_swap(asu(x), asu(x), false, false);
  x = asf(s[0]) + asf(s[1]);
  auto t = __builtin_amdgcn_permlane32_swap(asu(x), asu(x), false, false);
  return asf(t[0]) + asf(t[1]);
}
__device__ __forceinline__ float half_sum(float x) {
  x += dpp<XOR1>(x); x += dpp<XOR2>(x); x += dpp<HMIR>(x); return xrow_sum(x);
}
__device__ __forceinline__ float wave_sum(float x) { x = half_sum(x); return x + dpp<ROR8>(x); }
__device__ __forceinline__ float wave_max(float x) {
  x = fmaxf(x, dpp<XOR1>(x)); x = fmaxf(x, dpp<XOR2>(x)); x = fmaxf(x, dpp<HMIR>(x)); x = fmaxf(x, dpp<ROR8>(x)); return xrow_max(x);
}
__device__ __forceinline__ f32x4 mfma(u32x4 a, u32x4 b, f32x4 c) {
  return __builtin_amdgcn_mfma_f32_16x16x32_bf16(__builtin_bit_cast(bf16x8, a), __builtin_bit_cast(bf16x8, b), c, 0, 0, 0);
}
__device__ __forceinline__ rsrc_t mkrsrc(const void* p, u32 bytes) {
  return __builtin_amdgcn_make_buffer_rsrc((void*)p, 0, bytes, 0x00020000);
}
template <int AUX> __device__ __forceinline__ u32x4 bld(rsrc_t r, u32 voff, u32 soff) {
  return __builtin_amdgcn_raw_buffer_load_b128(r, voff, soff, AUX);
}
#define SB() __builtin_amdgcn_sched_barrier(0)
#define PIN(x) asm volatile("" : "+v"(x))
#define LGKM0() asm volatile("s_waitcnt lgkmcnt(0)" ::: "memory")
__device__ __forceinline__ void lds_barrier() { asm volatile("s_waitcnt lgkmcnt(0)\n\ts_barrier" ::: "memory"); }
template <int N> struct IC { static constexpr int v = N; };
__device__ __forceinline__ u32 norm2(float xv, float mb, float den, float r) {
  const float a = rbf(xv - mb), q0 = a * r, qq = fmaf(fmaf(-q0, den, a), r, q0);
  return cvtpk(qq, qq);
}

__global__ __launch_bounds__(NW * 64, 4) void fused_decode(
    const u16* __restrict__ x, const u16* __restrict__ Kc, const u16* __restrict__ Vc, const u16* __restrict__ wq,
    const u16* __restrict__ wo, const u16* __restrict__ w1, const u16* __restrict__ b1, const u16* __restrict__ w2,
    const u16* __restrict__ b2, u16* __restrict__ out, unsigned char* __restrict__ ws) {
  __shared__ __attribute__((aligned(16))) unsigned char lds_raw[L_END];
  LAS unsigned char* L = (LAS unsigned char*)lds_raw;
#define STAMP(s)
#define STAMP_OUT()
  const int tid = threadIdx.x, lane = tid & 63, w = __builtin_amdgcn_readfirstlane(tid >> 6);
  const int g = lane >> 4, c = lane & 15;
  const int bid = blockIdx.x, h = (bid & 7) * 4 + ((bid >> 3) & 3), b = bid >> 5;
  const u32 l16 = lane * 16;
  u32 budget = SPIN_CAP;
  volatile LAS u32* FULL = (volatile LAS u32*)(L + L_FULL);
  volatile LAS u32* NEXT = (volatile LAS u32*)(L + L_NEXT);

  if (w == 0) {
    __builtin_amdgcn_s_setprio(3);
    const size_t bh = (size_t)(b * NH + h) * (SK * DH);
    const rsrc_t rq = mkrsrc(wq + (size_t)(64 * b) * (NH * DH) + h * DH, (64 * NH * DH - h * DH) * 2);
    const rsrc_t rk = mkrsrc(Kc + bh, SK * DH * 2), rv = mkrsrc(Vc + bh, SK * DH * 2);
    const u32 ring0 = (u32)(uintptr_t)L;
    const u32 qoff = g * (16 * NH * DH * 2) + c * 16;
#pragma unroll
    for (int i = 0; i < 16; i++)
      asm volatile("s_mov_b32 m0, %0\n\ts_nop 0\n\tbuffer_load_dwordx4 %1, %2, %3 offen nt lds"
                   :: "s"(ring0 + i * 1024), "v"(qoff), "s"(rq), "s"(i * (NH * DH * 2)) : "memory");
    const u32 fulladdr = ring0 + L_FULL + lane * 4;
    FULL[lane] = 0u;
#define DMA(off) "buffer_load_dwordx4 %0, %1, %2 offen offset:" #off " nt lds\n\t"
    auto quad = [&](rsrc_t r, u32 m0v, u32 soff, auto WAIT, u32 pub) {
      constexpr bool wait = decltype(WAIT)::v;
      constexpr int W = WIN - GRP;
      constexpr bool pub0 = W % 4 == 0;
#define M0 "s_mov_b32 m0, %3\n\ts_nop 0\n\t"
#define WT "s_waitcnt vmcnt(%6)\n\t"
#define PB "ds_max_u32 %4, %5\n\t"
#define OPS :: "v"(l16), "s"(r), "s"(soff), "s"(m0v), "v"(fulladdr), "v"(pub), "i"(W) : "memory"
      if constexpr (!wait) asm volatile(M0 DMA(0) DMA(1024) DMA(2048) DMA(3072) OPS);
      else if constexpr (GRP == 4) asm volatile(M0 WT DMA(0) DMA(1024) DMA(2048) DMA(3072) PB OPS);
      else if constexpr (pub0) asm volatile(M0 WT DMA(0) DMA(1024) PB WT DMA(2048) DMA(3072) OPS);
      else asm volatile(M0 WT DMA(0) DMA(1024) WT DMA(2048) DMA(3072) PB OPS);
    };
    static_assert(WIN % GRP == 0 && WIN % 2 == 0 && WIN <= 32 && (GRP == 2 || GRP == 4), "");
    constexpr int PUBOFF = (WIN - GRP) % 4 == 0 ? WIN - GRP : WIN - GRP - 2;
#pragma unroll
    for (int j = 0; j < 16; j += 4) quad(rk, ring0 + (K0 + j) * 1024, j * 1024, IC<0>{}, 0);
    lds_barrier();
    u32 limit = RING;
    auto phase = [&](rsrc_t r, u32 base, u32 j0) {
#pragma unroll 1
      for (u32 j = j0; j < 1024; j += 16) {
        const u32 n = base + j;
        if (__builtin_expect(n + 16 > limit, 0)) {
          for (u32 spins = 0; budget; spins++, budget--) {
            const u32x4 nx = *(volatile LAS u32x4*)NEXT;
            limit = __builtin_amdgcn_readfirstlane(min(min(nx[0], nx[1]), min(nx[2], nx[3]))) + RING;
            if (n + 16 <= limit) break;
            if (spins == 0) { asm volatile("s_waitcnt vmcnt(0)" ::: "memory"); FULL[lane] = n; }
            __builtin_amdgcn_s_sleep(1);
          }
        }
        const u32 m0v = ring0 + (n & (RING - 1)) * 1024;
#pragma unroll
        for (int k = 0; k < 16; k += 4) quad(r, m0v + k * 1024, (j + k) * 1024, IC<1>{}, n + k - PUBOFF);
      }
    };
    phase(rk, K0, 16);
    phase(rv, V0, 0);
#pragma unroll
    for (int k = (WIN - GRP) / 4 * 4; k >= 0; k -= 4)
      asm volatile("s_waitcnt vmcnt(%2)\n\tds_max_u32 %0, %1" :: "v"(fulladdr), "v"(END - k), "i"(k) : "memory");
    LGKM0();
    STAMP_OUT();
    return;
  }

  GAS u32* ep = (GAS u32*)ws;
  u32 tag = __hip_atomic_load(ep, RLX_AGENT);
  u32x4 m4, m8;
#pragma unroll
  for (int r = 0; r < 4; r++) {
    const u32 mk = (((c & 7) >> 1) == r) ? ((c & 1) ? 0xffff0000u : 0x0000ffffu) : 0u;
    m8[r] = mk; m4[r] = c < 8 ? mk : 0u;
  }
  auto wait_full = [&](u32 need) {
    for (; budget; budget--) {
      if (__builtin_amdgcn_readfirstlane(FULL[0]) >= need) break;
      __builtin_amdgcn_s_sleep(1);
    }
    asm volatile("" ::: "memory");
  };
  constexpr float SCALE = 0.08838834764831845f, L2E = 1.4426950408889634f;

  if (w <= NC) {
    const int cw = w - 1;
    const rsrc_t rx = mkrsrc(x + (2 * cw) * DM, 2 * DM * 2);
    const u32 xb = (c >> 3) * (DM * 2);
    const u32x4 s0 = bld<0>(rx, xb + (g * 8 + (c & 7)) * 32, 0), s1 = bld<0>(rx, xb + (g * 8 + (c & 7)) * 32 + 16, 0);
    const u32x4 xa = bld<0>(rx, xb + g * 32, b * 128), xc = bld<0>(rx, xb + g * 32 + 16, b * 128);
    if (lane == 0) { NEXT[cw] = 0u; ((volatile LAS u32*)(L + L_MF))[cw] = 0u; }
    lds_barrier();
    PIN(tag); tag += 1u;
    u32x4 t[16];
    {
      float s = 0.f;
#pragma unroll
      for (int j = 0; j < 4; j++) s += (blo(s0[j]) + bhi(s0[j])) + (blo(s1[j]) + bhi(s1[j]));
      const float mean = half_sum(s) * (1.f / DM);
      float q = 0.f;
#pragma unroll
      for (int j = 0; j < 4; j++) {
        const float d0 = blo(s0[j]) - mean, d1 = bhi(s0[j]) - mean, d2 = blo(s1[j]) - mean, d3 = bhi(s1[j]) - mean;
        q = fmaf(d0, d0, q); q = fmaf(d1, d1, q); q = fmaf(d2, d2, q); q = fmaf(d3, d3, q);
      }
      q = half_sum(q);
      const float mb = rbf(mean), den = rbf(sqrtf(rbf(rbf(q * (1.f / DM)) + 1e-5f))), r = 1.f / den;
      u32 xn2[16];
#pragma unroll
      for (int i = 0; i < 16; i++) {
        const u32 wd = i < 8 ? (u32)xa[(i >> 1) & 3] : (u32)xc[(i >> 1) & 3];
        xn2[i] = norm2((i & 1) ? bhi(wd) : blo(wd), mb, den, r);
        PIN(xn2[i]);
      }
      wait_full(K0);
#pragma unroll
      for (int i = 0; i < 16; i++) t[i] = *(const LAS u32x4*)(L + i * 1024 + l16);
      LGKM0();
      NEXT[cw] = K0 + 16 * cw;
      f32x4 acc = {0.f, 0.f, 0.f, 0.f};
#pragma unroll
      for (int i = 0; i < 16; i++) acc = mfma(m8 & xn2[i], t[i], acc);
      const rsrc_t rqs = mkrsrc(ws + WS_Q + (size_t)(h * 8 + 2 * cw) * 8192 + b * 1024, 8192 + 1024);
      const u32 o = (g >> 1) * 8192 + (lane & 31) * 16;
      __builtin_amdgcn_raw_buffer_store_b128(u32x4{asu(acc[0]), tag, asu(acc[1]), tag}, rqs, o, 0, 16);
      __builtin_amdgcn_raw_buffer_store_b128(u32x4{asu(acc[2]), tag, asu(acc[3]), tag}, rqs, o + 512, 0, 16);
    }
    STAMP(st1);
    u32x4 qp;
    float mx = -INFINITY;
#pragma unroll 1
    for (int i = 0; i < 16; i++) {
      const u32 n = K0 + 16 * (cw + NC * i);
      wait_full(n + 16);
      const LAS unsigned char* src = L + (n & (RING - 1)) * 1024 + l16;
#pragma unroll
      for (int k = 0; k < 16; k++) t[k] = *(const LAS u32x4*)(src + k * 1024);
      LGKM0();
      NEXT[cw] = n + 16 * NC;
      if (i == 0) {
        for (; budget; budget--) {
          if (__builtin_amdgcn_readfirstlane(*(volatile LAS u32*)(L + L_QRDY))) break;
          __builtin_amdgcn_s_sleep(1);
        }
        asm volatile("" ::: "memory");
        qp = *(const LAS u32x4*)(L + L_Q + c * 16);
        STAMP(st2);
      }
      float s[16];
#pragma unroll
      for (int k = 0; k < 16; k++) {
        PIN(t[k]);
        float d = dot2(t[k][0], qp[0], 0.f);
        d = dot2(t[k][1], qp[1], d); d = dot2(t[k][2], qp[2], d); s[k] = dot2(t[k][3], qp[3], d);
      }
#define TR_STEP(HALF, CTRL, BIT) _Pragma("unroll") for (int k = 0; k < (HALF); k++) { \
        const float lo_ = s[k], hi_ = s[k + (HALF)]; const bool up_ = (c & (BIT)) != 0; \
        s[k] = (up_ ? hi_ : lo_) + dpp<CTRL>(up_ ? lo_ : hi_); }
      TR_STEP(8, ROR8, 8) TR_STEP(4, HMIR, 4) TR_STEP(2, XOR2, 2) TR_STEP(1, XOR1, 1)
#undef TR_STEP
      const float sc = rbf(rbf(s[0]) * SCALE);
      mx = fmaxf(mx, sc);
      *(LAS float*)(L + L_SC + ((cw + NC * i) * 64 + lane) * 4) = sc;
    }
    {
      mx = wave_max(mx);
      float e[16], ls = 0.f;
#pragma unroll
      for (int i = 0; i < 16; i++) {
        e[i] = __builtin_amdgcn_exp2f((*(const LAS float*)(L + L_SC + ((cw + NC * i) * 64 + lane) * 4) - mx) * L2E);
        ls += e[i];
      }
      ls = wave_sum(ls);
      if (lane == 0) { ((volatile LAS float*)(L + L_ML))[cw] = mx; ((volatile LAS float*)(L + L_ML))[4 + cw] = ls; }
      LGKM0();
      if (lane == 0) ((volatile LAS u32*)(L + L_MF))[cw] = 1u;
      for (; budget; budget--) {
        const u32x4 f = *(volatile LAS u32x4*)(L + L_MF);
        if (__builtin_amdgcn_readfirstlane(f[0] & f[1] & f[2] & f[3])) break;
        __builtin_amdgcn_s_sleep(1);
      }
      asm volatile("" ::: "memory");
      const f32x4 ma = *(volatile LAS f32x4*)(L + L_ML), la = *(volatile LAS f32x4*)(L + L_ML + 16);
      const float M = fmaxf(fmaxf(ma[0], ma[1]), fmaxf(ma[2], ma[3]));
      float Ls = 0.f;
#pragma unroll
      for (int i = 0; i < 4; i++) Ls = fmaf(__builtin_amdgcn_exp2f((ma[i] - M) * L2E), la[i], Ls);
      const float f = __builtin_amdgcn_exp2f((mx - M) * L2E) / Ls;
#pragma unroll
      for (int i = 0; i < 16; i++) { const float p = e[i] * f; *(LAS u32*)(L + L_SC + ((cw + NC * i) * 64 + lane) * 4) = cvtpk(p, p); }
      LGKM0();
    }
    STAMP(st3);
    f32x4 oacc = {0.f, 0.f, 0.f, 0.f};
#pragma unroll 1
    for (int i = 0; i < 16; i++) {
      const u32 n = V0 + 16 * (cw + NC * i);
      u32x4 pp[4];
#pragma unroll
      for (int k = 0; k < 4; k++) pp[k] = *(const LAS u32x4*)(L + L_SC + ((cw + NC * i) * 64 + g * 16 + 4 * k) * 4);
      const LAS unsigned char* src = L + (n & (RING - 1)) * 1024 + l16;
#pragma unroll
      for (int sub = 0; sub < 4; sub++) {
        wait_full(n + 4 * sub + 4);
#pragma unroll
        for (int k = 0; k < 4; k++) t[k] = *(const LAS u32x4*)(src + (4 * sub + k) * 1024);
        if (sub == 3) { LGKM0(); NEXT[cw] = n + 16 * NC; }
#pragma unroll
        for (int k = 0; k < 4; k++) oacc = mfma(m4 & pp[sub][k], t[k], oacc);
        PIN(oacc);
      }
    }
    if (lane < 32) *(LAS f32x4*)(L + L_OP + (cw * DH + 8 * c + 4 * g) * 4) = oacc;
    lds_barrier();
    STAMP_OUT();
    return;
  }

  const int hw = w - 1 - NC;
  const bool has_mlp = h < MLPH;
  const int hm = has_mlp ? h : 0;
  if (lane == 0) { ((volatile LAS u32*)(L + L_HF))[hw] = 0u; if (hw == 0) *(volatile LAS u32*)(L + L_QRDY) = 0u; }
  lds_barrier();
  PIN(tag); tag += 1u;
  if (hw == 0) {
    const rsrc_t rqs = mkrsrc(ws + WS_Q + (size_t)(h * 8 + b) * 8192, 8192);
    u32x4 gv[8];
    for (; budget; budget--) {
      bool ok = true;
      asm volatile("" ::: "memory");
#pragma unroll
      for (int i = 0; i < 8; i++) { gv[i] = bld<16>(rqs, l16, i * 1024); ok &= gv[i][1] == tag && gv[i][3] == tag; }
      if (__all(ok)) break;
      __builtin_amdgcn_s_sleep(4);
    }
    float q0 = 0.f, q1 = 0.f;
#pragma unroll
    for (int i = 0; i < 8; i++) { q0 += asf(gv[i][0]); q1 += asf(gv[i][2]); }
    *(LAS u32*)(L + L_Q + (4 * c + 2 * (g & 1) + (g >> 1)) * 4) = cvtpk(q0, q1);
    LGKM0();
    if (lane == 0) *(volatile LAS u32*)(L + L_QRDY) = 1u;
    STAMP(st1);
  }
  f32x4 y[4];
  u32x4 wr[16];
  const u32 off2 = g * (DM * 2) + c * 16;
#define PAUSE() do { asm volatile("s_waitcnt vmcnt(0)" ::: "memory"); __builtin_amdgcn_s_sleep(HSLEEP); } while (0)
  {
    for (int i = 0; i < 3; i++) __builtin_amdgcn_s_sleep(64);
    for (int i = 0; i < hw; i++) __builtin_amdgcn_s_sleep(8);
    const rsrc_t rx = mkrsrc(x + b * DM, DM * 2);
    const rsrc_t r1 = mkrsrc(w1 + hm * 64, NOSIDE ? 0 : (DM * FF - hm * 64) * 2);
    const rsrc_t r2 = mkrsrc(w2 + (size_t)hm * 64 * DM, NOSIDE ? 0 : 64 * DM * 2);
    const u32x4 x8 = bld<0>(rx, l16, 0), xa = bld<0>(rx, (lane >> 3) * 16, hw * 128);
    const float b1v = asf((u32)b1[64 * hm + 8 * hw + 4 * (c & 1) + g] << 16);
    const u32 off1 = (lane >> 3) * (8 * FF * 2) + (lane & 7) * 16;
#pragma unroll
    for (int i = 0; i < 8; i++) { wr[i] = bld<WAUX>(r1, off1, (64 * hw + i) * (FF * 2)); if (i & 1) PAUSE(); }
    float s = 0.f;
#pragma unroll
    for (int j = 0; j < 4; j++) s += blo(x8[j]) + bhi(x8[j]);
    const float mean = wave_sum(s) * (1.f / DM);
    float q = 0.f;
#pragma unroll
    for (int j = 0; j < 4; j++) { const float d0 = blo(x8[j]) - mean, d1 = bhi(x8[j]) - mean; q = fmaf(d0, d0, q); q = fmaf(d1, d1, q); }
    q = wave_sum(q);
    const float mb = rbf(mean), den = rbf(sqrtf(rbf(rbf(q * (1.f / DM)) + 1e-5f))), r = 1.f / den;
    f32x4 acc = {0.f, 0.f, 0.f, 0.f};
#pragma unroll
    for (int i = 0; i < 8; i++) acc = mfma(m8 & norm2((i & 1) ? bhi(xa[i >> 1]) : blo(xa[i >> 1]), mb, den, r), wr[i], acc);
    if ((g >> 1) == (c >> 3)) *(LAS f32x4*)(L + L_HP + ((2 * hw + (g >> 1)) * 64 + 8 * (c & 7) + 4 * (g & 1)) * 4) = acc;
    LGKM0();
    if (lane == 0) ((volatile LAS u32*)(L + L_HF))[hw] = 1u;
#pragma unroll
    for (int i = 0; i < 8; i++) { wr[i] = bld<WAUX>(r2, off2 + (i >> 1) * 256, (8 * hw + 4 * (i & 1)) * (DM * 2)); if (i & 1) PAUSE(); }
    for (; budget; budget--) {
      const u32x4 f0 = *(volatile LAS u32x4*)(L + L_HF), f1 = *(volatile LAS u32x4*)(L + L_HF + 16);
      if (__builtin_amdgcn_readfirstlane(f0[0] & f0[1] & f0[2] & f0[3] & f1[0] & f1[1] & f1[2] & f1[3])) break;
      __builtin_amdgcn_s_sleep(4);
    }
    asm volatile("" ::: "memory");
    const int u = 8 * hw + 4 * (c & 1) + g;
    float hs = *(const LAS float*)(L + L_HP + ((2 * (c >> 1)) * 64 + u) * 4) + *(const LAS float*)(L + L_HP + ((2 * (c >> 1) + 1) * 64 + u) * 4);
    hs += dpp<XOR2>(hs); hs += dpp<ROR4>(hs); hs += dpp<ROR8>(hs);
    const float hv = rbf(rbf(hs) + b1v);
    const float e = __builtin_amdgcn_exp2f((2.f * L2E * 0.7978845608028654f) * (hv + 0.044715f * hv * hv * hv));
    const float th = 1.f - 2.f / (e + 1.f);
    float gl = 0.5f * hv * (1.f + th);
    gl = has_mlp ? gl : 0.f;
    const u32 g2 = cvtpk(gl, gl);
    const u32 ga = dppu<0x00>(g2), gb = dppu<0x55>(g2);
#pragma unroll
    for (int cb = 0; cb < 4; cb++) {
      f32x4 a = {0.f, 0.f, 0.f, 0.f};
      a = mfma(m4 & ga, wr[2 * cb], a);
      y[cb] = mfma(m4 & gb, wr[2 * cb + 1], a);
      PIN(y[cb]);
    }
    const rsrc_t ro = mkrsrc(wo + (size_t)h * DH * DM, NOSIDE ? 0 : DH * DM * 2);
#pragma unroll
    for (int i = 0; i < 16; i++) { wr[i] = bld<WAUX>(ro, off2 + (i >> 2) * 256, (16 * hw + 4 * (i & 3)) * (DM * 2)); if (i & 1) PAUSE(); }
#pragma unroll
    for (int i = 0; i < 16; i++) PIN(wr[i]);
  }
  float base = 0.f;
  if (hw == 0) base = asf((u32)x[b * DM + 16 * h + c] << 16) + asf((u32)b2[16 * h + c] << 16);
  PIN(base);
  STAMP(st2);
  lds_barrier();
  {
    const int d = 16 * hw + 4 * (c & 3) + g;
    float o = *(const LAS float*)(L + L_OP + ((c >> 2) * DH + d) * 4);
    o += dpp<ROR4>(o); o += dpp<ROR8>(o);
    const u32 o2 = cvtpk(o, o);
    u32 oq[4];
    oq[0] = dppu<0x00>(o2); oq[1] = dppu<0x55>(o2); oq[2] = dppu<0xAA>(o2); oq[3] = dppu<0xFF>(o2);
#pragma unroll
    for (int i = 0; i < 16; i++) y[i >> 2] = mfma(m4 & oq[i & 3], wr[i], y[i >> 2]);
    if (lane < 32) {
#pragma unroll
      for (int cb = 0; cb < 4; cb++) *(LAS f32x4*)(L + L_YP + (hw * DM + 128 * cb + 8 * c + 4 * g) * 4) = y[cb];
    }
  }
  lds_barrier();
  {
    const int col = hw * 64 + lane;
    float s = *(const LAS float*)(L + L_YP + col * 4);
#pragma unroll
    for (int i = 1; i < NHOLD; i++) s += *(const LAS float*)(L + L_YP + (i * DM + col) * 4);
    GAS u64* slot = (GAS u64*)(ws + WS_Y) + ((size_t)(b * 32 + (col >> 4)) * 32 + h) * 16 + (col & 15);
    __hip_atomic_store(slot, ((u64)tag << 32) | asu(s), RLX_AGENT);
  }
  STAMP(st3);
  if (hw == 0) {
    GAS u64* src = (GAS u64*)(ws + WS_Y) + ((size_t)(b * 32 + h) * 32 + g) * 16 + c;
    u64 gv[8];
    for (; budget; budget--) {
      bool ok = true;
#pragma unroll
      for (int i = 0; i < 8; i++) { gv[i] = __hip_atomic_load(src + i * 64, RLX_AGENT); ok &= (u32)(gv[i] >> 32) == tag; }
      if (__all(ok)) break;
      __builtin_amdgcn_s_sleep(4);
    }
    float s = 0.f;
#pragma unroll
    for (int i = 0; i < 8; i++) s += asf((u32)gv[i]);
    s = xrow_sum(s) + base;
    if (lane < 16) out[b * DM + 16 * h + c] = (u16)(cvtpk(s, s) & 0xffffu);
    if (bid == 0 && lane == 0) __hip_atomic_store(ep, tag, RLX_AGENT);
  }
  STAMP_OUT();
}

extern "C" void kernel_launch(void* const* d_in, const int* in_sizes, int n_in, void* d_out, int out_size, void* d_ws,
                              size_t ws_size, hipStream_t stream) {
  fused_decode<<<NB * NH, NW * 64, 0, stream>>>((const u16*)d_in[0], (const u16*)d_in[1], (const u16*)d_in[2], (const u16*)d_in[3],
                                                (const u16*)d_in[4], (const u16*)d_in[6], (const u16*)d_in[7], (const u16*)d_in[8],
                                                (const u16*)d_in[9], (u16*)d_out, (unsigned char*)d_ws);
}
```

```cpp
#include <hip/hip_runtime.h>
#include <cstdint>

typedef unsigned short u16;
typedef unsigned u32;
typedef unsigned long long u64;
typedef u32 u32x4 __attribute__((ext_vector_type(4)));
typedef u32 u32x2 __attribute__((ext_vector_type(2)));
typedef float f32x4 __attribute__((ext_vector_type(4)));
typedef short bf16x8 __attribute__((ext_vector_type(8)));
typedef __bf16 bf16x2 __attribute__((ext_vector_type(2)));
typedef __amdgpu_buffer_rsrc_t rsrc_t;
#define GAS __attribute__((address_space(1)))
#define LAS __attribute__((address_space(3)))
#define RLX_AGENT __ATOMIC_RELAXED, __HIP_MEMORY_SCOPE_AGENT

#define WIN 20
#define WIN0 WIN
#define WINK WIN
#define WINT WIN
#define TAILK 128
#define HEADK 192
#define WARM 0
#define QSLEEP 8
#define T0 800
#define SLOT 300
#define BURST 4
#define ROT 1
#define WA 20
#define WB 22
#define WC 26
#define WD 30
#define SCH_A 80
#define SCH_T 897
#define LB 40
#define LC 100
#define DRY 0
#define YSLEEP 4
#define NOQ 0
#define VSLEEP 1
#define CODEKB 0
#define GRP 2
#define WAUX 0
#define NOSIDE 0
#define HSLEEP 40
#define SN1 144
#define SN2 528
#define SN3 V0
#define STAMPS 0

constexpr int NB = 8, DM = 512, NH = 32, SK = 4096, DH = 128, FF = 1792;
constexpr int NC = 4, NHOLD = 8, NW = 1 + NC + NHOLD;
constexpr int MLPH = FF / 64;
constexpr int RING = 128;
constexpr int K0 = 16, V0 = K0 + 1024, END = V0 + 1024;
constexpr size_t WS_Y = 65536, WS_Q = WS_Y + 8 * 32 * 2048, WS_STAMP = 8u << 20;
constexpr u32 QM = 0x3fu, YM = 0xffu;
constexpr u32 SPIN_CAP = 1u << 18;
constexpr int L_RING = 0, L_SC = RING * 1024, L_HP = L_SC + SK * 4, L_OP = L_HP + 2 * NHOLD * 64 * 4, L_Q = L_OP + NC * DH * 4;
constexpr int L_FULL = L_Q + 256, L_NEXT = L_FULL + 256, L_QRDY = L_NEXT + 16, L_ML = L_QRDY + 16, L_MF = L_ML + 32, L_PF = L_MF + 16, L_HF = L_PF + 16, L_END = L_HF + 32;
constexpr int L_YP = 0;

__device__ __forceinline__ float asf(u32 v) { return __builtin_bit_cast(float, v); }
__device__ __forceinline__ u32 asu(float v) { return __builtin_bit_cast(u32, v); }
__device__ __forceinline__ float blo(u32 v) { return asf(v << 16); }
__device__ __forceinline__ float bhi(u32 v) { return asf(v & 0xffff0000u); }
__device__ __forceinline__ u32 cvtpk(float lo, float hi) {
  bf16x2 r = {(__bf16)lo, (__bf16)hi};
  return __builtin_bit_cast(u32, r);
}
__device__ __forceinline__ float rbf(float x) { return (float)(__bf16)x; }
__device__ __forceinline__ float dot2(u32 a, u32 b, float c) {
  return __builtin_amdgcn_fdot2_f32_bf16(__builtin_bit_cast(bf16x2, a), __builtin_bit_cast(bf16x2, b), c, false);
}
template <int CTRL> __device__ __forceinline__ float dpp(float x) {
  return asf(__builtin_amdgcn_mov_dpp(asu(x), CTRL, 0xf, 0xf, true));
}
template <int CTRL> __device__ __forceinline__ u32 dppu(u32 x) { return __builtin_amdgcn_mov_dpp(x, CTRL, 0xf, 0xf, true); }
constexpr int XOR1 = 0xB1, XOR2 = 0x4E, HMIR = 0x141, ROR8 = 0x128, ROR4 = 0x124;
__device__ __forceinline__ float xrow_max(float x) {
  auto s = __builtin_amdgcn_permlane16_swap(asu(x), asu(x), false, false);
  x = fmaxf(asf(s[0]), asf(s[1]));
  auto t = __builtin_amdgcn_permlane32_swap(asu(x), asu(x), false, false);
  return fmaxf(asf(t[0]), asf(t[1]));
}
__device__ __forceinline__ float xrow_sum(float x) {
  auto s = __builtin_amdgcn_permlane16_swap(asu(x), asu(x), false, false);
  x = asf(s[0]) + asf(s[1]);
  auto t = __builtin_amdgcn_permlane32_swap(asu(x), asu(x), false, false);
  return asf(t[0]) + asf(t[1]);
}
__device__ __forceinline__ float half_sum(float x) {
  x += dpp<XOR1>(x); x += dpp<XOR2>(x); x += dpp<HMIR>(x); return xrow_sum(x);
}
__device__ __forceinline__ float wave_sum(float x) { x = half_sum(x); return x + dpp<ROR8>(x); }
__device__ __forceinline__ float wave_max(float x) {
  x = fmaxf(x, dpp<XOR1>(x)); x = fmaxf(x, dpp<XOR2>(x)); x = fmaxf(x, dpp<HMIR>(x)); x = fmaxf(x, dpp<ROR8>(x)); return xrow_max(x);
}
__device__ __forceinline__ f32x4 mfma(u32x4 a, u32x4 b, f32x4 c) {
  return __builtin_amdgcn_mfma_f32_16x16x32_bf16(__builtin_bit_cast(bf16x8, a), __builtin_bit_cast(bf16x8, b), c, 0, 0, 0);
}
__device__ __forceinline__ rsrc_t mkrsrc(const void* p, u32 bytes) {
  return __builtin_amdgcn_make_buffer_rsrc((void*)p, 0, bytes, 0x00020000);
}
template <int AUX> __device__ __forceinline__ u32x4 bld(rsrc_t r, u32 voff, u32 soff) {
  return __builtin_amdgcn_raw_buffer_load_b128(r, voff, soff, AUX);
}
#define SB() __builtin_amdgcn_sched_barrier(0)
#define PIN(x) asm volatile("" : "+v"(x))
#define LGKM0() asm volatile("s_waitcnt lgkmcnt(0)" ::: "memory")
__device__ __forceinline__ void lds_barrier() { asm volatile("s_waitcnt lgkmcnt(0)\n\ts_barrier" ::: "memory"); }
template <int N> struct IC { static constexpr int v = N; };
template <u32 M> __device__ __forceinline__ u32x4 tagged(f32x4 v, u32 tag) {
  u32x4 r;
#pragma unroll
  for (int i = 0; i < 4; i++) r[i] = ((asu(v[i]) + (M + 1) / 2) & ~M) | (tag & M);
  return r;
}
template <u32 M> __device__ __forceinline__ bool fresh(u32x4 v, u32 tag) {
  return (((v[0] ^ tag) | (v[1] ^ tag) | (v[2] ^ tag) | (v[3] ^ tag)) & M) == 0u;
}
__device__ __forceinline__ u32 norm2(float xv, float mb, float den, float r) {
  const float a = rbf(xv - mb), q0 = a * r, qq = fmaf(fmaf(-q0, den, a), r, q0);
  return cvtpk(qq, qq);
}

__global__ __launch_bounds__(NW * 64, 4) void fused_decode(
    const u16* __restrict__ x, const u16* __restrict__ Kc, const u16* __restrict__ Vc, const u16* __restrict__ wq,
    const u16* __restrict__ wo, const u16* __restrict__ w1, const u16* __restrict__ b1, const u16* __restrict__ w2,
    const u16* __restrict__ b2, u16* __restrict__ out, unsigned char* __restrict__ ws) {
  __shared__ __attribute__((aligned(16))) unsigned char lds_raw[L_END];
  LAS unsigned char* L = (LAS unsigned char*)lds_raw;
#define STAMP(s)
#define STAMP_OUT()
  const u32 tstart = (u32)__builtin_amdgcn_s_memrealtime();
  const int tid = threadIdx.x, lane = tid & 63, w = __builtin_amdgcn_readfirstlane(tid >> 6);
  const int g = lane >> 4, c = lane & 15;
  const int bid = blockIdx.x, h = (bid & 7) * 4 + ((bid >> 3) & 3), b = bid >> 5;
  const u32 l16 = lane * 16;
  u32 budget = SPIN_CAP;
  u32x4 cpf = {0u, 0u, 0u, 0u};
  if (CODEKB && w == NW - 1) {
    const size_t pc = __builtin_amdgcn_s_getpc() & ~(size_t)255;
    cpf = bld<0>(mkrsrc((const void*)pc, CODEKB * 1024), l16 + (bid >> 3) * 1024, 0);
  }
  volatile LAS u32* FULL = (volatile LAS u32*)(L + L_FULL);
  volatile LAS u32* NEXT = (volatile LAS u32*)(L + L_NEXT);

  if (__builtin_expect(w == 0, 1)) {
    __builtin_amdgcn_s_setprio(3);
    const size_t bh = (size_t)(b * NH + h) * (SK * DH);
    const int qc = h * DH + 64 * (b & 1);
    const rsrc_t rq = mkrsrc(wq + (size_t)(128 * (b >> 1)) * (NH * DH) + qc, (128 * NH * DH - qc) * 2);
    const rsrc_t rk = mkrsrc(Kc + bh, SK * DH * 2), rv = mkrsrc(Vc + bh, SK * DH * 2);
    const u32 ring0 = (u32)(uintptr_t)L;
    const u32 qoff = (lane >> 3) * (16 * NH * DH * 2) + (lane & 7) * 16;
#pragma unroll
    for (int i = 0; i < 16; i++)
      asm volatile("s_mov_b32 m0, %0\n\ts_nop 0\n\tbuffer_load_dwordx4 %1, %2, %3 offen nt lds"
                   :: "s"(ring0 + i * 1024), "v"(qoff), "s"(rq), "s"(i * (NH * DH * 2)) : "memory");
    const u32 fulladdr = ring0 + L_FULL + lane * 4;
    FULL[lane] = 0u;
#define DMA(off) "buffer_load_dwordx4 %0, %1, %2 offen offset:" #off " nt lds\n\t"
    auto quad = [&](rsrc_t r, u32 m0v, u32 soff, auto WINDOW, u32 pos) {
      constexpr int win = decltype(WINDOW)::v;
      constexpr bool wait = win != 0;
      constexpr int W = wait ? win - GRP : 0;
      constexpr bool pub0 = W % 4 == 0;
      static_assert(win % GRP == 0 && win % 2 == 0 && win <= 34 && (GRP == 2 || GRP == 4), "");
      const u32 pub = pos - (pub0 ? W : W - 2);
#define M0 "s_mov_b32 m0, %3\n\ts_nop 0\n\t"
#define WT "s_waitcnt vmcnt(%6)\n\t"
#define PB "ds_max_u32 %4, %5\n\t"
#define OPS :: "v"(l16), "s"(r), "s"(soff), "s"(m0v), "v"(fulladdr), "v"(pub), "i"(W) : "memory"
      if constexpr (!wait) asm volatile(M0 DMA(0) DMA(1024) DMA(2048) DMA(3072) OPS);
      else if constexpr (GRP == 4) asm volatile(M0 WT DMA(0) DMA(1024) DMA(2048) DMA(3072) PB OPS);
      else if constexpr (pub0) asm volatile(M0 WT DMA(0) DMA(1024) PB WT DMA(2048) DMA(3072) OPS);
      else asm volatile(M0 WT DMA(0) DMA(1024) WT DMA(2048) DMA(3072) PB OPS);
    };
#pragma unroll
    for (int j = 0; j < 16; j += 4) quad(rk, ring0 + (K0 + j) * 1024, j * 1024, IC<0>{}, 0);
    lds_barrier();
    u32 limit = RING;
    int late = -1;
    auto phase = [&](rsrc_t r, u32 base, u32 j0) {
#pragma unroll 1
      for (u32 j = j0; j < 1024; j += 16) {
        const u32 n = base + j;
        if (__builtin_expect(n + 16 > limit, 0)) {
_Pragma("unroll 1")
          for (u32 spins = 0; budget; spins++, budget--) {
            const u32x4 nx = *(volatile LAS u32x4*)NEXT;
            limit = __builtin_amdgcn_readfirstlane(min(min(nx[0], nx[1]), min(nx[2], nx[3]))) + RING;
            if (n + 16 <= limit) break;
            if (spins == 0) { asm volatile("s_waitcnt vmcnt(0)" ::: "memory"); FULL[lane] = n; }
            __builtin_amdgcn_s_sleep(1);
          }
        }
        const u32 m0v = ring0 + (n & (RING - 1)) * 1024;
        u64 now = __builtin_amdgcn_s_memrealtime();
        auto body = [&](auto WINDOW) {
#pragma unroll
          for (int k = 0; k < 16; k += 4) quad(r, m0v + k * 1024, (j + k) * 1024, WINDOW, n + k);
        };
        if (late < 0) body(IC<WA>{});
        else if (late < LB) body(IC<WB>{});
        else if (late < LC) body(IC<WC>{});
        else body(IC<WD>{});
        asm volatile("" : "+s"(now));
        late = (int)((u32)now - tstart) - (int)(SCH_A + ((n * SCH_T) >> 8));
      }
    };
    phase(rk, K0, 16);
    phase(rv, V0, 0);
#pragma unroll
    for (int k = (WD - GRP) / 4 * 4; k >= 0; k -= 4)
      asm volatile("s_waitcnt vmcnt(%2)\n\tds_max_u32 %0, %1" :: "v"(fulladdr), "v"(END - k), "i"(k) : "memory");
    LGKM0();
    STAMP_OUT();
    __builtin_amdgcn_endpgm();
  }

  GAS u32* ep = (GAS u32*)ws;
  u32 tag = __hip_atomic_load(ep, RLX_AGENT);
  u32x4 m4, m8;
#pragma unroll
  for (int r = 0; r < 4; r++) {
    const u32 mk = (((c & 7) >> 1) == r) ? ((c & 1) ? 0xffff0000u : 0x0000ffffu) : 0u;
    m8[r] = mk; m4[r] = c < 8 ? mk : 0u;
  }
  auto wait_full = [&](u32 need) {
_Pragma("unroll 1")
    for (; budget; budget--) {
      if (__builtin_amdgcn_readfirstlane(FULL[0]) >= need) break;
      __builtin_amdgcn_s_sleep(1);
    }
    asm volatile("" ::: "memory");
  };
  constexpr float SCALE = 0.08838834764831845f, L2E = 1.4426950408889634f;

  if (w <= NC) {
    const int cw = w - 1;
    const rsrc_t rx = mkrsrc(x + (2 * cw) * DM, 2 * DM * 2);
    const u32 xb = (c >> 3) * (DM * 2);
    u32x4 s0 = bld<0>(rx, xb + (g * 8 + (c & 7)) * 32, 0), s1 = bld<0>(rx, xb + (g * 8 + (c & 7)) * 32 + 16, 0);
    u32x2 xq = __builtin_amdgcn_raw_buffer_load_b64(rx, xb + (g * 8 + (c & 7)) * 8, (b >> 1) * 256, 0);
    if (lane == 0) { NEXT[cw] = 0u; ((volatile LAS u32*)(L + L_MF))[cw] = 0u; ((volatile LAS u32*)(L + L_PF))[cw] = 0u; }
    lds_barrier();
    PIN(tag); PIN(s0); PIN(s1); PIN(xq);
    tag += 1u;
    u32x4 t[16];
    {
      float s = 0.f;
#pragma unroll
      for (int j = 0; j < 4; j++) s += (blo(s0[j]) + bhi(s0[j])) + (blo(s1[j]) + bhi(s1[j]));
      const float mean = half_sum(s) * (1.f / DM);
      float q = 0.f;
#pragma unroll
      for (int j = 0; j < 4; j++) {
        const float d0 = blo(s0[j]) - mean, d1 = bhi(s0[j]) - mean, d2 = blo(s1[j]) - mean, d3 = bhi(s1[j]) - mean;
        q = fmaf(d0, d0, q); q = fmaf(d1, d1, q); q = fmaf(d2, d2, q); q = fmaf(d3, d3, q);
      }
      q = half_sum(q);
      const float mb = rbf(mean), den = rbf(__builtin_amdgcn_sqrtf(rbf(rbf(q * (1.f / DM)) + 1e-5f))), r = __builtin_amdgcn_rcpf(den);
      LAS unsigned char* xl = L + L_SC + cw * 1024 + (c >> 3) * 512;
      *(LAS u32x4*)(xl + (g * 8 + (c & 7)) * 16) =
          u32x4{norm2(blo(xq[0]), mb, den, r), norm2(bhi(xq[0]), mb, den, r), norm2(blo(xq[1]), mb, den, r), norm2(bhi(xq[1]), mb, den, r)};
      LGKM0();
      wait_full(K0);
      f32x4 a0 = {0.f, 0.f, 0.f, 0.f}, a1 = a0;
#pragma unroll 1
      for (int i = 0; i < 4; i++) {
        const u32x4 e0 = *(const LAS u32x4*)(xl + g * 128 + i * 16), e1 = *(const LAS u32x4*)(xl + g * 128 + 64 + i * 16);
#pragma unroll
        for (int k = 0; k < 4; k++) t[k] = *(const LAS u32x4*)(L + (4 * i + k) * 1024 + l16);
#pragma unroll
        for (int k = 0; k < 4; k++) { a0 = mfma(m8 & e0[k], t[k], a0); a1 = mfma(m8 & e1[k], t[k], a1); }
      }
      LGKM0();
      NEXT[cw] = K0 + 16 * cw;
      f32x4 z;
#pragma unroll
      for (int i = 0; i < 4; i++) { const float u = c < 8 ? a0[i] : a1[i]; z[i] = u + dpp<ROR8>(u); }
      const rsrc_t rqs = mkrsrc(ws + WS_Q + (size_t)(h * 8 + 2 * cw) * 2048 + ((b & 1) * 4 + (b >> 1)) * 256, (NOQ & 1) ? 0 : 2048 + 256);
      if (c < 8) __builtin_amdgcn_raw_buffer_store_b128(tagged<QM>(z, tag), rqs, (g >> 1) * 2048 + (2 * c + (g & 1)) * 16, 0, 16);
    }
    STAMP(st1);
    u32x4 qp;
    float mx = -INFINITY;
#pragma unroll 1
    for (int i = 0; i < 16; i++) {
      const u32 n = K0 + 16 * (cw + NC * i);
      wait_full(n + 16);
      const LAS unsigned char* src = L + (n & (RING - 1)) * 1024 + l16;
#pragma unroll
      for (int k = 0; k < 16; k++) t[k] = *(const LAS u32x4*)(src + k * 1024);
      LGKM0();
      NEXT[cw] = i < 15 ? n + 16 * NC : V0 + 4 * cw;
      if (i == 0) {
_Pragma("unroll 1")
        for (; budget; budget--) {
          if (__builtin_amdgcn_readfirstlane(*(volatile LAS u32*)(L + L_QRDY))) break;
          __builtin_amdgcn_s_sleep(1);
        }
        asm volatile("" ::: "memory");
        qp = *(const LAS u32x4*)(L + L_Q + c * 16);
        STAMP(st2);
      }
      float s[16];
#pragma unroll
      for (int k = 0; k < 16; k++) {
        PIN(t[k]);
        float d = dot2(t[k][0], qp[0], 0.f);
        d = dot2(t[k][1], qp[1], d); d = dot2(t[k][2], qp[2], d); s[k] = dot2(t[k][3], qp[3], d);
      }
#define TR_STEP(HALF, CTRL, BIT) _Pragma("unroll") for (int k = 0; k < (HALF); k++) { \
        const float lo_ = s[k], hi_ = s[k + (HALF)]; const bool up_ = (c & (BIT)) != 0; \
        s[k] = (up_ ? hi_ : lo_) + dpp<CTRL>(up_ ? lo_ : hi_); }
      TR_STEP(8, ROR8, 8) TR_STEP(4, HMIR, 4) TR_STEP(2, XOR2, 2) TR_STEP(1, XOR1, 1)
#undef TR_STEP
      const float sc = rbf(rbf(s[0]) * SCALE);
      mx = fmaxf(mx, sc);
      *(LAS float*)(L + L_SC + ((cw + NC * i) * 64 + lane) * 4) = sc;
    }
    {
      mx = wave_max(mx);
      float e[16], ls = 0.f;
#pragma unroll
      for (int i = 0; i < 16; i++) {
        e[i] = __builtin_amdgcn_exp2f((*(const LAS float*)(L + L_SC + ((cw + NC * i) * 64 + lane) * 4) - mx) * L2E);
        ls += e[i];
      }
      ls = wave_sum(ls);
      if (lane == 0) { ((volatile LAS float*)(L + L_ML))[cw] = mx; ((volatile LAS float*)(L + L_ML))[4 + cw] = ls; }
      LGKM0();
      if (lane == 0) ((volatile LAS u32*)(L + L_MF))[cw] = 1u;
_Pragma("unroll 1")
      for (; budget; budget--) {
        const u32x4 f = *(volatile LAS u32x4*)(L + L_MF);
        if (__builtin_amdgcn_readfirstlane(f[0] & f[1] & f[2] & f[3])) break;
        __builtin_amdgcn_s_sleep(1);
      }
      asm volatile("" ::: "memory");
      const f32x4 ma = *(volatile LAS f32x4*)(L + L_ML), la = *(volatile LAS f32x4*)(L + L_ML + 16);
      const float M = fmaxf(fmaxf(ma[0], ma[1]), fmaxf(ma[2], ma[3]));
      float Ls = 0.f;
#pragma unroll
      for (int i = 0; i < 4; i++) Ls = fmaf(__builtin_amdgcn_exp2f((ma[i] - M) * L2E), la[i], Ls);
      const float f = __builtin_amdgcn_exp2f((mx - M) * L2E) / Ls;
#pragma unroll
      for (int i = 0; i < 16; i++) { const float p = e[i] * f; *(LAS u32*)(L + L_SC + ((cw + NC * i) * 64 + lane) * 4) = cvtpk(p, p); }
      LGKM0();
      if (lane == 0) ((volatile LAS u32*)(L + L_PF))[cw] = 1u;
_Pragma("unroll 1")
      for (; budget; budget--) {
        const u32x4 f = *(volatile LAS u32x4*)(L + L_PF);
        if (__builtin_amdgcn_readfirstlane(f[0] & f[1] & f[2] & f[3])) break;
        __builtin_amdgcn_s_sleep(1);
      }
      asm volatile("" ::: "memory");
    }
    STAMP(st3);
    f32x4 oacc = {0.f, 0.f, 0.f, 0.f};
#pragma unroll 1
    for (int i = 0; i < 64; i++) {
      const u32 n = V0 + 16 * i + 4 * cw;
      u32x4 pp = *(const LAS u32x4*)(L + L_SC + (i * 64 + g * 16 + 4 * cw) * 4);
      u32x4 pa[4];
#pragma unroll
      for (int k = 0; k < 4; k++) { pa[k] = m4 & pp[k]; PIN(pa[k]); }
      const LAS unsigned char* src = L + (n & (RING - 1)) * 1024 + l16;
_Pragma("unroll 1")
      for (; budget; budget--) {
        if (__builtin_amdgcn_readfirstlane(FULL[0]) >= n + 4) break;
        __builtin_amdgcn_s_sleep(VSLEEP);
      }
      asm volatile("" ::: "memory");
#pragma unroll
      for (int k = 0; k < 4; k++) t[k] = *(const LAS u32x4*)(src + k * 1024);
#pragma unroll
      for (int k = 0; k < 4; k++) oacc = mfma(pa[k], t[k], oacc);
      PIN(oacc);
      LGKM0();
      NEXT[cw] = n + 16;
    }
    if (lane < 32) *(LAS f32x4*)(L + L_OP + (cw * DH + 8 * c + 4 * g) * 4) = oacc;
    lds_barrier();
    STAMP_OUT();
    __builtin_amdgcn_endpgm();
  }

  const int hw = w - 1 - NC;
  const bool has_mlp = h < MLPH;
  const int hm = has_mlp ? h : 0;
  if (lane == 0) { ((volatile LAS u32*)(L + L_HF))[hw] = 0u; if (hw == 0) *(volatile LAS u32*)(L + L_QRDY) = 0u; }
  lds_barrier();
  PIN(tag); PIN(cpf); tag += 1u;
  if (hw == 0) {
    const rsrc_t rqs = mkrsrc(ws + WS_Q + (size_t)(h * 8 + b) * 2048, (NOQ & 2) ? 0 : 2048);
    u32x4 g0, g1;
_Pragma("unroll 1")
    for (; budget; budget--) {
      const u32x4 nx = *(volatile LAS u32x4*)NEXT;
      if (__builtin_amdgcn_readfirstlane(min(min(nx[0], nx[1]), min(nx[2], nx[3])))) break;
      __builtin_amdgcn_s_sleep(2);
    }
_Pragma("unroll 1")
    for (;; budget--) {
      __builtin_amdgcn_s_sleep(QSLEEP);
      asm volatile("" ::: "memory");
      g0 = bld<16>(rqs, l16, 0); g1 = bld<16>(rqs, l16 + 1024, 0);
      if (__all(fresh<QM>(g0, tag) && fresh<QM>(g1, tag)) || !budget || NOQ) break;
    }
    float qa[4], qb[4];
#pragma unroll
    for (int i = 0; i < 4; i++) { qa[i] = xrow_sum(asf(g0[i] & ~QM)); qb[i] = xrow_sum(asf(g1[i] & ~QM)); }
    if (g == 0) *(LAS u32x2*)(L + L_Q + c * 8) = u32x2{cvtpk(qa[0], qa[1]), cvtpk(qa[2], qa[3])};
    if (g == 1) *(LAS u32x2*)(L + L_Q + 128 + c * 8) = u32x2{cvtpk(qb[0], qb[1]), cvtpk(qb[2], qb[3])};
    LGKM0();
    if (lane == 0) *(volatile LAS u32*)(L + L_QRDY) = 1u;
    STAMP(st1);
  }
  f32x4 y[4];
  u32x4 wr[16];
  const u32 off2 = g * (DM * 2) + c * 16;
#define PAUSE(i) do { if (((i) + 1) % BURST == 0) asm volatile("s_waitcnt vmcnt(0)" ::: "memory"); } while (0)
  auto wait_slot = [&](int round) {
    const u32 due = T0 + (round * 8 + ((hw + (ROT ? b : 0)) & 7)) * SLOT;
_Pragma("unroll 1")
    for (; budget && (u32)__builtin_amdgcn_s_memrealtime() - tstart < due; budget--) __builtin_amdgcn_s_sleep(8);
  };
  {
    wait_slot(0);
    const rsrc_t rx = mkrsrc(x + b * DM, DM * 2);
    const rsrc_t r1 = mkrsrc(w1 + hm * 64, NOSIDE ? 0 : (DM * FF - hm * 64) * 2);
    const rsrc_t r2 = mkrsrc(w2 + (size_t)hm * 64 * DM, NOSIDE ? 0 : 64 * DM * 2);
    const u32x4 x8 = bld<0>(rx, l16, 0), xa = bld<0>(rx, (lane >> 3) * 16, hw * 128);
    const float b1v = asf((u32)b1[64 * hm + 8 * hw + 4 * (c & 1) + g] << 16);
    const u32 off1 = (lane >> 3) * (8 * FF * 2) + (lane & 7) * 16;
#pragma unroll
    for (int i = 0; i < 8; i++) { wr[i] = bld<WAUX>(r1, off1, (64 * hw + i) * (FF * 2)); PAUSE(i); }
#pragma unroll
    for (int i = 0; i < 8; i++) { wr[8 + i] = bld<WAUX>(r2, off2 + (i >> 1) * 256, (8 * hw + 4 * (i & 1)) * (DM * 2)); PAUSE(i); }
    float s = 0.f;
#pragma unroll
    for (int j = 0; j < 4; j++) s += blo(x8[j]) + bhi(x8[j]);
    const float mean = wave_sum(s) * (1.f / DM);
    float q = 0.f;
#pragma unroll
    for (int j = 0; j < 4; j++) { const float d0 = blo(x8[j]) - mean, d1 = bhi(x8[j]) - mean; q = fmaf(d0, d0, q); q = fmaf(d1, d1, q); }
    q = wave_sum(q);
    const float mb = rbf(mean), den = rbf(__builtin_amdgcn_sqrtf(rbf(rbf(q * (1.f / DM)) + 1e-5f))), r = __builtin_amdgcn_rcpf(den);
    f32x4 acc = {0.f, 0.f, 0.f, 0.f};
#pragma unroll
    for (int i = 0; i < 8; i++) acc = mfma(m8 & norm2((i & 1) ? bhi(xa[i >> 1]) : blo(xa[i >> 1]), mb, den, r), wr[i], acc);
    if ((g >> 1) == (c >> 3)) *(LAS f32x4*)(L + L_HP + ((2 * hw + (g >> 1)) * 64 + 8 * (c & 7) + 4 * (g & 1)) * 4) = acc;
    LGKM0();
    if (lane == 0) ((volatile LAS u32*)(L + L_HF))[hw] = 1u;
_Pragma("unroll 1")
    for (; budget; budget--) {
      const u32x4 f0 = *(volatile LAS u32x4*)(L + L_HF), f1 = *(volatile LAS u32x4*)(L + L_HF + 16);
      if (__builtin_amdgcn_readfirstlane(f0[0] & f0[1] & f0[2] & f0[3] & f1[0] & f1[1] & f1[2] & f1[3])) break;
      __builtin_amdgcn_s_sleep(16);
    }
    asm volatile("" ::: "memory");
    const int u = 8 * hw + 4 * (c & 1) + g;
    float hs = *(const LAS float*)(L + L_HP + ((2 * (c >> 1)) * 64 + u) * 4) + *(const LAS float*)(L + L_HP + ((2 * (c >> 1) + 1) * 64 + u) * 4);
    hs += dpp<XOR2>(hs); hs += dpp<ROR4>(hs); hs += dpp<ROR8>(hs);
    const float hv = rbf(rbf(hs) + b1v);
    const float e = __builtin_amdgcn_exp2f((2.f * L2E * 0.7978845608028654f) * (hv + 0.044715f * hv * hv * hv));
    const float th = 1.f - 2.f / (e + 1.f);
    float gl = 0.5f * hv * (1.f + th);
    gl = has_mlp ? gl : 0.f;
    const u32 g2 = cvtpk(gl, gl);
    const u32 ga = dppu<0x00>(g2), gb = dppu<0x55>(g2);
#pragma unroll
    for (int cb = 0; cb < 4; cb++) {
      f32x4 a = {0.f, 0.f, 0.f, 0.f};
      a = mfma(m4 & ga, wr[8 + 2 * cb], a);
      y[cb] = mfma(m4 & gb, wr[9 + 2 * cb], a);
      PIN(y[cb]);
    }
    wait_slot(1);
    const rsrc_t ro = mkrsrc(wo + (size_t)h * DH * DM, NOSIDE ? 0 : DH * DM * 2);
#pragma unroll
    for (int i = 0; i < 16; i++) { wr[i] = bld<WAUX>(ro, off2 + (i >> 2) * 256, (16 * hw + 4 * (i & 3)) * (DM * 2)); PAUSE(i); }
#pragma unroll
    for (int i = 0; i < 16; i++) PIN(wr[i]);
  }
  f32x4 base = {0.f, 0.f, 0.f, 0.f};
  if (hw == 0) {
    const u32x2 xv = *(const u32x2*)(x + b * DM + 16 * h + 4 * (lane & 3)), bv = *(const u32x2*)(b2 + 16 * h + 4 * (lane & 3));
    base = f32x4{blo(xv[0]) + blo(bv[0]), bhi(xv[0]) + bhi(bv[0]), blo(xv[1]) + blo(bv[1]), bhi(xv[1]) + bhi(bv[1])};
  }
  PIN(base);
  const rsrc_t ry = mkrsrc(ws + WS_Y + (size_t)(b * 32 + 4 * hw) * 2048 + h * 64, 3 * 2048 + 64);
  const rsrc_t rs = mkrsrc(ws + WS_Y + (size_t)(b * 32 + h) * 2048, 2048);
  STAMP(st2);
  lds_barrier();
  {
    const int d = 16 * hw + 4 * (c & 3) + g;
    float o = *(const LAS float*)(L + L_OP + ((c >> 2) * DH + d) * 4);
    o += dpp<ROR4>(o); o += dpp<ROR8>(o);
    const u32 o2 = cvtpk(o, o);
    u32 oq[4];
    oq[0] = dppu<0x00>(o2); oq[1] = dppu<0x55>(o2); oq[2] = dppu<0xAA>(o2); oq[3] = dppu<0xFF>(o2);
#pragma unroll
    for (int i = 0; i < 16; i++) y[i >> 2] = mfma(m4 & oq[i & 3], wr[i], y[i >> 2]);
    if (lane < 32) {
#pragma unroll
      for (int cb = 0; cb < 4; cb++) *(LAS f32x4*)(L + L_YP + (hw * DM + 128 * cb + 8 * c + 4 * g) * 4) = y[cb];
    }
  }
  lds_barrier();
  {
    const LAS unsigned char* yp = L + L_YP + (2 * g * DM + 64 * hw + 4 * c) * 4;
    f32x4 s = *(const LAS f32x4*)yp + *(const LAS f32x4*)(yp + DM * 4);
#pragma unroll
    for (int i = 0; i < 4; i++) s[i] = xrow_sum(s[i]);
    if (g == 0) __builtin_amdgcn_raw_buffer_store_b128(tagged<YM>(s, tag), ry, (c >> 2) * 2048 + (c & 3) * 16, 0, 16);
  }
  STAMP(st3);
  if (hw == 0) {
    u32x4 g0, g1;
_Pragma("unroll 1")
    for (;; budget--) {
      asm volatile("" ::: "memory");
      g0 = bld<16>(rs, l16, 0); g1 = bld<16>(rs, l16 + 1024, 0);
      if (__all(fresh<YM>(g0, tag) && fresh<YM>(g1, tag)) || !budget) break;
      __builtin_amdgcn_s_sleep(YSLEEP);
    }
    float s[4];
#pragma unroll
    for (int i = 0; i < 4; i++) {
      float u = asf(g0[i] & ~YM) + asf(g1[i] & ~YM);
      u += dpp<ROR4>(u); u += dpp<ROR8>(u);
      s[i] = xrow_sum(u) + base[i];
    }
    if (lane < 4) *(u32x2*)(out + b * DM + 16 * h + 4 * lane) = u32x2{cvtpk(s[0], s[1]), cvtpk(s[2], s[3])};
    if (bid == 0 && lane == 0) __hip_atomic_store(ep, tag, RLX_AGENT);
  }
  STAMP_OUT();
  __builtin_amdgcn_endpgm();
}

extern "C" void kernel_launch(void* const* d_in, const int* in_sizes, int n_in, void* d_out, int out_size, void* d_ws,
                              size_t ws_size, hipStream_t stream) {
  fused_decode<<<NB * NH, NW * 64, 0, stream>>>((const u16*)d_in[0], (const u16*)d_in[1], (const u16*)d_in[2], (const u16*)d_in[3],
                                                (const u16*)d_in[4], (const u16*)d_in[6], (const u16*)d_in[7], (const u16*)d_in[8],
                                                (const u16*)d_in[9], (u16*)d_out, (unsigned char*)d_ws);
}
```

```cpp
#include <hip/hip_runtime.h>
#include <cstdint>

typedef unsigned short u16;
typedef unsigned u32;
typedef unsigned long long u64;
typedef u32 u32x4 __attribute__((ext_vector_type(4)));
typedef u32 u32x2 __attribute__((ext_vector_type(2)));
typedef float f32x4 __attribute__((ext_vector_type(4)));
typedef short bf16x8 __attribute__((ext_vector_type(8)));
typedef __bf16 bf16x2 __attribute__((ext_vector_type(2)));
typedef __amdgpu_buffer_rsrc_t rsrc_t;
#define GAS __attribute__((address_space(1)))
#define LAS __attribute__((address_space(3)))
#define RLX_AGENT __ATOMIC_RELAXED, __HIP_MEMORY_SCOPE_AGENT

#define WIN 20
#define WIN0 WIN
#define WINK WIN
#define WINT WIN
#define TAILK 128
#define HEADK 192
#define WARM 0
#define QSLEEP 8
#define T0 800
#define SLOT 300
#define BURST 4
#define ROT 1
#define WA 18
#define WB 20
#define WC 22
#define WD 26
#define WE 30
#define SCH_A 10
#define SCH_T 897
#define LB 40
#define LC 80
#define LD 130
#define DRY 0
#define YSLEEP 0
#define NOQ 0
#define VSLEEP 1
#define CODEKB 0
#define PEER 1
#define SCH_M 60
#define CKP 256
#define PAUX 16
#define WARMB 1
#define YPIPE 0
#define YFIRST 0
#define YGAP 6
#define CKEND 200
#define HEARLY 0
#define GRP 2
#define WAUX 0
#define NOSIDE 0
#define HSLEEP 40
#define SN1 144
#define SN2 528
#define SN3 V0
#define STAMPS 0

constexpr int NB = 8, DM = 512, NH = 32, SK = 4096, DH = 128, FF = 1792;
constexpr int NC = 4, NHOLD = 8, NW = 1 + NC + NHOLD + 1;
constexpr int MLPH = FF / 64;
constexpr int RING = 128;
constexpr int K0 = 16, V0 = K0 + 1024, END = V0 + 1024;
constexpr size_t WS_Y = 65536, WS_Q = WS_Y + 8 * 32 * 2048, WS_P = 4096, WS_STAMP = 8u << 20;
constexpr u32 QM = 0x3fu, YM = 0xffu;
constexpr u32 SPIN_CAP = 1u << 18;
constexpr int OPS_ = DH * 2 + 16;
constexpr int L_RING = 0, L_SC = RING * 1024, L_HP = L_SC + SK * 4, L_OP = L_HP + 2 * NHOLD * 64 * 4, L_HV = L_OP + 2 * NC * OPS_, L_Q = L_HV + NHOLD * 128;
constexpr int L_FULL = L_Q + 256, L_NEXT = L_FULL + 256, L_QRDY = L_NEXT + 16, L_ML = L_QRDY + 16, L_MF = L_ML + 32, L_PF = L_MF + 16, L_HF = L_PF + 16, L_T0 = L_HF + 32, L_END = L_T0 + 16;

__device__ __forceinline__ float asf(u32 v) { return __builtin_bit_cast(float, v); }
__device__ __forceinline__ u32 asu(float v) { return __builtin_bit_cast(u32, v); }
__device__ __forceinline__ float blo(u32 v) { return asf(v << 16); }
__device__ __forceinline__ float bhi(u32 v) { return asf(v & 0xffff0000u); }
__device__ __forceinline__ u32 cvtpk(float lo, float hi) {
  bf16x2 r = {(__bf16)lo, (__bf16)hi};
  return __builtin_bit_cast(u32, r);
}
__device__ __forceinline__ float rbf(float x) { return (float)(__bf16)x; }
__device__ __forceinline__ float dot2(u32 a, u32 b, float c) {
  return __builtin_amdgcn_fdot2_f32_bf16(__builtin_bit_cast(bf16x2, a), __builtin_bit_cast(bf16x2, b), c, false);
}
template <int CTRL> __device__ __forceinline__ float dpp(float x) {
  return asf(__builtin_amdgcn_mov_dpp(asu(x), CTRL, 0xf, 0xf, true));
}
template <int CTRL> __device__ __forceinline__ u32 dppu(u32 x) { return __builtin_amdgcn_mov_dpp(x, CTRL, 0xf, 0xf, true); }
constexpr int XOR1 = 0xB1, XOR2 = 0x4E, HMIR = 0x141, ROR8 = 0x128, ROR4 = 0x124;
__device__ __forceinline__ float xrow_max(float x) {
  auto s = __builtin_amdgcn_permlane16_swap(asu(x), asu(x), false, false);
  x = fmaxf(asf(s[0]), asf(s[1]));
  auto t = __builtin_amdgcn_permlane32_swap(asu(x), asu(x), false, false);
  return fmaxf(asf(t[0]), asf(t[1]));
}
__device__ __forceinline__ float xrow_sum(float x) {
  auto s = __builtin_amdgcn_permlane16_swap(asu(x), asu(x), false, false);
  x = asf(s[0]) + asf(s[1]);
  auto t = __builtin_amdgcn_permlane32_swap(asu(x), asu(x), false, false);
  return asf(t[0]) + asf(t[1]);
}
__device__ __forceinline__ float half_sum(float x) {
  x += dpp<XOR1>(x); x += dpp<XOR2>(x); x += dpp<HMIR>(x); return xrow_sum(x);
}
__device__ __forceinline__ float wave_sum(float x) { x = half_sum(x); return x + dpp<ROR8>(x); }
__device__ __forceinline__ float wave_max(float x) {
  x = fmaxf(x, dpp<XOR1>(x)); x = fmaxf(x, dpp<XOR2>(x)); x = fmaxf(x, dpp<HMIR>(x)); x = fmaxf(x, dpp<ROR8>(x)); return xrow_max(x);
}
__device__ __forceinline__ f32x4 mfma(u32x4 a, u32x4 b, f32x4 c) {
  return __builtin_amdgcn_mfma_f32_16x16x32_bf16(__builtin_bit_cast(bf16x8, a), __builtin_bit_cast(bf16x8, b), c, 0, 0, 0);
}
__device__ __forceinline__ rsrc_t mkrsrc(const void* p, u32 bytes) {
  return __builtin_amdgcn_make_buffer_rsrc((void*)p, 0, bytes, 0x00020000);
}
template <int AUX> __device__ __forceinline__ u32x4 bld(rsrc_t r, u32 voff, u32 soff) {
  return __builtin_amdgcn_raw_buffer_load_b128(r, voff, soff, AUX);
}
#define SB() __builtin_amdgcn_sched_barrier(0)
#define PIN(x) asm volatile("" : "+v"(x))
#define LGKM0() asm volatile("s_waitcnt lgkmcnt(0)" ::: "memory")
__device__ __forceinline__ void lds_barrier() { asm volatile("s_waitcnt lgkmcnt(0)\n\ts_barrier" ::: "memory"); }
template <int N> struct IC { static constexpr int v = N; };
template <u32 M> __device__ __forceinline__ u32x4 tagged(f32x4 v, u32 tag) {
  u32x4 r;
#pragma unroll
  for (int i = 0; i < 4; i++) r[i] = ((asu(v[i]) + (M + 1) / 2) & ~M) | (tag & M);
  return r;
}
template <u32 M> __device__ __forceinline__ bool fresh(u32x4 v, u32 tag) {
  return (((v[0] ^ tag) | (v[1] ^ tag) | (v[2] ^ tag) | (v[3] ^ tag)) & M) == 0u;
}
__device__ __forceinline__ u32 norm2(float xv, float mb, float den, float r) {
  const float a = rbf(xv - mb), q0 = a * r, qq = fmaf(fmaf(-q0, den, a), r, q0);
  return cvtpk(qq, qq);
}

__global__ __launch_bounds__(NW * 64, 4) void fused_decode(
    const u16* __restrict__ x, const u16* __restrict__ Kc, const u16* __restrict__ Vc, const u16* __restrict__ wq,
    const u16* __restrict__ wo, const u16* __restrict__ w1, const u16* __restrict__ b1, const u16* __restrict__ w2,
    const u16* __restrict__ b2, u16* __restrict__ out, unsigned char* __restrict__ ws) {
  __shared__ __attribute__((aligned(16))) unsigned char lds_raw[L_END];
  LAS unsigned char* L = (LAS unsigned char*)lds_raw;
#define STAMP(s)
#define STAMP_OUT()
  const u32 tstart = (u32)__builtin_amdgcn_s_memrealtime();
  const int tid = threadIdx.x, lane = tid & 63, w = __builtin_amdgcn_readfirstlane(tid >> 6);
  const int g = lane >> 4, c = lane & 15;
  const int bid = blockIdx.x, h = (bid & 7) * 4 + ((bid >> 3) & 3), b = bid >> 5;
  const u32 l16 = lane * 16;
  u32 budget = SPIN_CAP;
  u32x4 cpf = {0u, 0u, 0u, 0u};
  if (CODEKB && w == NW - 1) {
    const size_t pc = __builtin_amdgcn_s_getpc() & ~(size_t)255;
    cpf = bld<0>(mkrsrc((const void*)pc, CODEKB * 1024), l16 + ((bid >> 3) & 15) * 1024, 0);
  }
  volatile LAS u32* FULL = (volatile LAS u32*)(L + L_FULL);
  volatile LAS u32* NEXT = (volatile LAS u32*)(L + L_NEXT);

  if (__builtin_expect(w == 0, 1)) {
    __builtin_amdgcn_s_setprio(3);
    const size_t bh = (size_t)(b * NH + h) * (SK * DH);
    const int qc = h * DH + 64 * (b & 1);
    const rsrc_t rq = mkrsrc(wq + (size_t)(128 * (b >> 1)) * (NH * DH) + qc, (128 * NH * DH - qc) * 2);
    const rsrc_t rk = mkrsrc(Kc + bh, SK * DH * 2);
    const u32 ring0 = (u32)(uintptr_t)L;
    const u32 qoff = (lane >> 3) * (16 * NH * DH * 2) + (lane & 7) * 16;
#pragma unroll
    for (int i = 0; i < 16; i++)
      asm volatile("s_mov_b32 m0, %0\n\ts_nop 0\n\tbuffer_load_dwordx4 %1, %2, %3 offen nt lds"
                   :: "s"(ring0 + i * 1024), "v"(qoff), "s"(rq), "s"(i * (NH * DH * 2)) : "memory");
    const u32 fulladdr = ring0 + L_FULL + lane * 4;
    FULL[lane] = 0u;
#define DMA(off) "buffer_load_dwordx4 %0, %1, %2 offen offset:" #off " nt lds\n\t"
    auto quad = [&](rsrc_t r, u32 m0v, u32 soff, auto WINDOW, u32 pos) {
      constexpr int win = decltype(WINDOW)::v;
      constexpr bool wait = win != 0;
      constexpr int W = wait ? win - GRP : 0;
      constexpr bool pub0 = W % 4 == 0;
      static_assert(win % GRP == 0 && win % 2 == 0 && win <= 34 && (GRP == 2 || GRP == 4), "");
      const u32 pub = pos - (pub0 ? W : W - 2);
#define M0 "s_mov_b32 m0, %3\n\ts_nop 0\n\t"
#define WT "s_waitcnt vmcnt(%6)\n\t"
#define PB "ds_max_u32 %4, %5\n\t"
#define OPS :: "v"(l16), "s"(r), "s"(soff), "s"(m0v), "v"(fulladdr), "v"(pub), "i"(W) : "memory"
      if constexpr (!wait) asm volatile(M0 DMA(0) DMA(1024) DMA(2048) DMA(3072) OPS);
      else if constexpr (GRP == 4) asm volatile(M0 WT DMA(0) DMA(1024) DMA(2048) DMA(3072) PB OPS);
      else if constexpr (pub0) asm volatile(M0 WT DMA(0) DMA(1024) PB WT DMA(2048) DMA(3072) OPS);
      else asm volatile(M0 WT DMA(0) DMA(1024) WT DMA(2048) DMA(3072) PB OPS);
    };
#pragma unroll
    for (int j = 0; j < 16; j += 4) quad(rk, ring0 + (K0 + j) * 1024, j * 1024, IC<0>{}, 0);
    lds_barrier();
    u32 limit = RING;
    int late = WARMB ? LD : -1;
    {
#pragma unroll 1
      for (u32 n = K0 + 16; n < END; n += 16) {
        const u32 j = (n - K0) & 1023;
        const rsrc_t r = mkrsrc((n < V0 ? Kc : Vc) + bh, SK * DH * 2);
        if (__builtin_expect(n + 16 > limit, 0)) {
_Pragma("unroll 1")
          for (u32 spins = 0; budget; spins++, budget--) {
            const u32x4 nx = *(volatile LAS u32x4*)NEXT;
            limit = __builtin_amdgcn_readfirstlane(min(min(nx[0], nx[1]), min(nx[2], nx[3]))) + RING;
            if (n + 16 <= limit) break;
            if (spins == 0) { asm volatile("s_waitcnt vmcnt(0)" ::: "memory"); FULL[lane] = n; }
            __builtin_amdgcn_s_sleep(1);
          }
        }
        const u32 m0v = ring0 + (n & (RING - 1)) * 1024;
        u64 now = __builtin_amdgcn_s_memrealtime();
        u32 t0v = *(volatile LAS u32*)(L + L_T0);
        auto body = [&](auto WINDOW) {
#pragma unroll
          for (int k = 0; k < 16; k += 4) quad(r, m0v + k * 1024, (j + k) * 1024, WINDOW, n + k);
        };
        if (late < 0) body(IC<WA>{});
        else if (late < LB) body(IC<WB>{});
        else if (late < LC) body(IC<WC>{});
        else if (late < LD) body(IC<WD>{});
        else body(IC<WE>{});
        asm volatile("" : "+s"(now));
        PIN(t0v);
        late = (int)((u32)now - __builtin_amdgcn_readfirstlane(t0v) - ((n * SCH_T) >> 8));
        if (WARMB && n < K0 + 80) late = n == K0 + 16 ? LC : n == K0 + 32 ? LB : n == K0 + 48 ? -1 : 0;
      }
    }
#pragma unroll
    for (int k = (WE - GRP) / 4 * 4; k >= 0; k -= 4)
      asm volatile("s_waitcnt vmcnt(%2)\n\tds_max_u32 %0, %1" :: "v"(fulladdr), "v"(END - k), "i"(k) : "memory");
    LGKM0();
    STAMP_OUT();
    __builtin_amdgcn_endpgm();
  }

  GAS u32* ep = (GAS u32*)ws;
  u32 tag = __hip_atomic_load(ep, RLX_AGENT);
  u32x4 m4, m8;
#pragma unroll
  for (int r = 0; r < 4; r++) {
    const u32 mk = (((c & 7) >> 1) == r) ? ((c & 1) ? 0xffff0000u : 0x0000ffffu) : 0u;
    m8[r] = mk; m4[r] = c < 8 ? mk : 0u;
  }
  auto wait_full = [&](u32 need) {
_Pragma("unroll 1")
    for (; budget; budget--) {
      if (__builtin_amdgcn_readfirstlane(FULL[0]) >= need) break;
      __builtin_amdgcn_s_sleep(1);
    }
    asm volatile("" ::: "memory");
  };
  constexpr float SCALE = 0.08838834764831845f, L2E = 1.4426950408889634f;

  if (w <= NC) {
    const int cw = w - 1;
    const rsrc_t rx = mkrsrc(x + (2 * cw) * DM, 2 * DM * 2);
    const u32 xb = (c >> 3) * (DM * 2);
    u32x4 s0 = bld<0>(rx, xb + (g * 8 + (c & 7)) * 32, 0), s1 = bld<0>(rx, xb + (g * 8 + (c & 7)) * 32 + 16, 0);
    u32x2 xq = __builtin_amdgcn_raw_buffer_load_b64(rx, xb + (g * 8 + (c & 7)) * 8, (b >> 1) * 256, 0);
    if (lane == 0) { NEXT[cw] = 0u; ((volatile LAS u32*)(L + L_MF))[cw] = 0u; ((volatile LAS u32*)(L + L_PF))[cw] = 0u; }
    lds_barrier();
    PIN(tag); PIN(s0); PIN(s1); PIN(xq);
    tag += 1u;
    u32x4 t[16];
    {
      float s = 0.f;
#pragma unroll
      for (int j = 0; j < 4; j++) s += (blo(s0[j]) + bhi(s0[j])) + (blo(s1[j]) + bhi(s1[j]));
      const float mean = half_sum(s) * (1.f / DM);
      float q = 0.f;
#pragma unroll
      for (int j = 0; j < 4; j++) {
        const float d0 = blo(s0[j]) - mean, d1 = bhi(s0[j]) - mean, d2 = blo(s1[j]) - mean, d3 = bhi(s1[j]) - mean;
        q = fmaf(d0, d0, q); q = fmaf(d1, d1, q); q = fmaf(d2, d2, q); q = fmaf(d3, d3, q);
      }
      q = half_sum(q);
      const float mb = rbf(mean), den = rbf(__builtin_amdgcn_sqrtf(rbf(rbf(q * (1.f / DM)) + 1e-5f))), r = __builtin_amdgcn_rcpf(den);
      LAS unsigned char* xl = L + L_SC + cw * 1024 + (c >> 3) * 512;
      *(LAS u32x4*)(xl + (g * 8 + (c & 7)) * 16) =
          u32x4{norm2(blo(xq[0]), mb, den, r), norm2(bhi(xq[0]), mb, den, r), norm2(blo(xq[1]), mb, den, r), norm2(bhi(xq[1]), mb, den, r)};
      LGKM0();
      wait_full(K0);
      f32x4 a0 = {0.f, 0.f, 0.f, 0.f}, a1 = a0;
#pragma unroll 1
      for (int i = 0; i < 4; i++) {
        const u32x4 e0 = *(const LAS u32x4*)(xl + g * 128 + i * 16), e1 = *(const LAS u32x4*)(xl + g * 128 + 64 + i * 16);
#pragma unroll
        for (int k = 0; k < 4; k++) t[k] = *(const LAS u32x4*)(L + (4 * i + k) * 1024 + l16);
#pragma unroll
        for (int k = 0; k < 4; k++) { a0 = mfma(m8 & e0[k], t[k], a0); a1 = mfma(m8 & e1[k], t[k], a1); }
      }
      LGKM0();
      NEXT[cw] = K0 + 16 * cw;
      f32x4 z;
#pragma unroll
      for (int i = 0; i < 4; i++) { const float u = c < 8 ? a0[i] : a1[i]; z[i] = u + dpp<ROR8>(u); }
      const rsrc_t rqs = mkrsrc(ws + WS_Q + (size_t)(h * 8 + 2 * cw) * 2048 + ((b & 1) * 4 + (b >> 1)) * 256, (NOQ & 1) ? 0 : 2048 + 256);
      if (c < 8) __builtin_amdgcn_raw_buffer_store_b128(tagged<QM>(z, tag), rqs, (g >> 1) * 2048 + (2 * c + (g & 1)) * 16, 0, 16);
    }
    STAMP(st1);
    u32x4 qp;
    float mx = -INFINITY;
#pragma unroll 1
    for (int i = 0; i < 16; i++) {
      const u32 n = K0 + 16 * (cw + NC * i);
      wait_full(n + 16);
      const LAS unsigned char* src = L + (n & (RING - 1)) * 1024 + l16;
#pragma unroll
      for (int k = 0; k < 16; k++) t[k] = *(const LAS u32x4*)(src + k * 1024);
      LGKM0();
      NEXT[cw] = i < 15 ? n + 16 * NC : V0 + 4 * cw;
      if (i == 0) {
_Pragma("unroll 1")
        for (; budget; budget--) {
          if (__builtin_amdgcn_readfirstlane(*(volatile LAS u32*)(L + L_QRDY))) break;
          __builtin_amdgcn_s_sleep(1);
        }
        asm volatile("" ::: "memory");
        qp = *(const LAS u32x4*)(L + L_Q + c * 16);
        STAMP(st2);
      }
      float s[16];
#pragma unroll
      for (int k = 0; k < 16; k++) {
        PIN(t[k]);
        float d = dot2(t[k][0], qp[0], 0.f);
        d = dot2(t[k][1], qp[1], d); d = dot2(t[k][2], qp[2], d); s[k] = dot2(t[k][3], qp[3], d);
      }
#define TR_STEP(HALF, CTRL, BIT) _Pragma("unroll") for (int k = 0; k < (HALF); k++) { \
        const float lo_ = s[k], hi_ = s[k + (HALF)]; const bool up_ = (c & (BIT)) != 0; \
        s[k] = (up_ ? hi_ : lo_) + dpp<CTRL>(up_ ? lo_ : hi_); }
      TR_STEP(8, ROR8, 8) TR_STEP(4, HMIR, 4) TR_STEP(2, XOR2, 2) TR_STEP(1, XOR1, 1)
#undef TR_STEP
      const float sc = rbf(rbf(s[0]) * SCALE);
      mx = fmaxf(mx, sc);
      *(LAS float*)(L + L_SC + ((cw + NC * i) * 64 + lane) * 4) = sc;
    }
    {
      mx = wave_max(mx);
      float e[16], ls = 0.f;
#pragma unroll
      for (int i = 0; i < 16; i++) {
        e[i] = __builtin_amdgcn_exp2f((*(const LAS float*)(L + L_SC + ((cw + NC * i) * 64 + lane) * 4) - mx) * L2E);
        ls += e[i];
      }
      ls = wave_sum(ls);
      if (lane == 0) { ((volatile LAS float*)(L + L_ML))[cw] = mx; ((volatile LAS float*)(L + L_ML))[4 + cw] = ls; }
      LGKM0();
      if (lane == 0) ((volatile LAS u32*)(L + L_MF))[cw] = 1u;
_Pragma("unroll 1")
      for (; budget; budget--) {
        const u32x4 f = *(volatile LAS u32x4*)(L + L_MF);
        if (__builtin_amdgcn_readfirstlane(f[0] & f[1] & f[2] & f[3])) break;
        __builtin_amdgcn_s_sleep(1);
      }
      asm volatile("" ::: "memory");
      const f32x4 ma = *(volatile LAS f32x4*)(L + L_ML), la = *(volatile LAS f32x4*)(L + L_ML + 16);
      const float M = fmaxf(fmaxf(ma[0], ma[1]), fmaxf(ma[2], ma[3]));
      float Ls = 0.f;
#pragma unroll
      for (int i = 0; i < 4; i++) Ls = fmaf(__builtin_amdgcn_exp2f((ma[i] - M) * L2E), la[i], Ls);
      const float f = __builtin_amdgcn_exp2f((mx - M) * L2E) / Ls;
#pragma unroll
      for (int i = 0; i < 16; i++) { const float p = e[i] * f; *(LAS u32*)(L + L_SC + ((cw + NC * i) * 64 + lane) * 4) = cvtpk(p, p); }
      LGKM0();
      if (lane == 0) ((volatile LAS u32*)(L + L_PF))[cw] = 1u;
_Pragma("unroll 1")
      for (; budget; budget--) {
        const u32x4 f = *(volatile LAS u32x4*)(L + L_PF);
        if (__builtin_amdgcn_readfirstlane(f[0] & f[1] & f[2] & f[3])) break;
        __builtin_amdgcn_s_sleep(1);
      }
      asm volatile("" ::: "memory");
    }
    STAMP(st3);
    f32x4 oacc = {0.f, 0.f, 0.f, 0.f};
#pragma unroll 1
    for (int i = 0; i < 64; i++) {
      const u32 n = V0 + 16 * i + 4 * cw;
      u32x4 pp = *(const LAS u32x4*)(L + L_SC + (i * 64 + g * 16 + 4 * cw) * 4);
      u32x4 pa[4];
#pragma unroll
      for (int k = 0; k < 4; k++) { pa[k] = m4 & pp[k]; PIN(pa[k]); }
      const LAS unsigned char* src = L + (n & (RING - 1)) * 1024 + l16;
_Pragma("unroll 1")
      for (; budget; budget--) {
        if (__builtin_amdgcn_readfirstlane(FULL[0]) >= n + 4) break;
        __builtin_amdgcn_s_sleep(VSLEEP);
      }
      asm volatile("" ::: "memory");
#pragma unroll
      for (int k = 0; k < 4; k++) t[k] = *(const LAS u32x4*)(src + k * 1024);
#pragma unroll
      for (int k = 0; k < 4; k++) oacc = mfma(pa[k], t[k], oacc);
      PIN(oacc);
      LGKM0();
      NEXT[cw] = n + 16;
    }
    {
      const u32x2 hi = {cvtpk(oacc[0], oacc[1]), cvtpk(oacc[2], oacc[3])};
      const u32x2 lo = {cvtpk(oacc[0] - blo(hi[0]), oacc[1] - bhi(hi[0])), cvtpk(oacc[2] - blo(hi[1]), oacc[3] - bhi(hi[1]))};
      LAS unsigned char* op = L + L_OP + 2 * cw * OPS_ + (8 * c + 4 * g) * 2;
      if (lane < 32) { *(LAS u32x2*)op = hi; *(LAS u32x2*)(op + OPS_) = lo; }
    }
    lds_barrier();
    STAMP_OUT();
    __builtin_amdgcn_endpgm();
  }

  if (w == NW - 1) {
    u32x2 xv = *(const u32x2*)(x + b * DM + 16 * h + 4 * (lane & 3)), bv = *(const u32x2*)(b2 + 16 * h + 4 * (lane & 3));
    if (lane == 0) { *(volatile LAS u32*)(L + L_QRDY) = 0u; *(volatile LAS u32*)(L + L_T0) = tstart + SCH_A; }
    lds_barrier();
    PIN(tag); PIN(cpf); PIN(xv); PIN(bv); tag += 1u;
    const rsrc_t rp = mkrsrc(ws + WS_P, (END / CKP + 1) * 512);
    if (PEER && lane == bid) __builtin_amdgcn_raw_buffer_store_b64(u32x2{tstart, tag}, rp, lane * 8, 0, 16);
    u32x4 g0, g1;
    {
      const rsrc_t rqs = mkrsrc(ws + WS_Q + (size_t)(h * 8 + b) * 2048, (NOQ & 2) ? 0 : 2048);
_Pragma("unroll 1")
      for (; budget; budget--) {
        const u32x4 nx = *(volatile LAS u32x4*)NEXT;
        if (__builtin_amdgcn_readfirstlane(min(min(nx[0], nx[1]), min(nx[2], nx[3])))) break;
        __builtin_amdgcn_s_sleep(2);
      }
_Pragma("unroll 1")
      for (;; budget--) {
        __builtin_amdgcn_s_sleep(QSLEEP);
        asm volatile("" ::: "memory");
        g0 = bld<16>(rqs, l16, 0); g1 = bld<16>(rqs, l16 + 1024, 0);
        if (__all(fresh<QM>(g0, tag) && fresh<QM>(g1, tag)) || !budget || NOQ) break;
      }
      float qa[4], qb[4];
#pragma unroll
      for (int i = 0; i < 4; i++) { qa[i] = xrow_sum(asf(g0[i] & ~QM)); qb[i] = xrow_sum(asf(g1[i] & ~QM)); }
      if (g == 0) *(LAS u32x2*)(L + L_Q + c * 8) = u32x2{cvtpk(qa[0], qa[1]), cvtpk(qa[2], qa[3])};
      if (g == 1) *(LAS u32x2*)(L + L_Q + 128 + c * 8) = u32x2{cvtpk(qb[0], qb[1]), cvtpk(qb[2], qb[3])};
      LGKM0();
      if (lane == 0) *(volatile LAS u32*)(L + L_QRDY) = 1u;
      STAMP(st1);
    }
    {
      u32 fixed = tstart + SCH_A;
_Pragma("unroll 1")
      for (u32 k = 1; k * CKP < END - CKEND; k++) {
_Pragma("unroll 1")
        for (; budget; budget--) {
          if (__builtin_amdgcn_readfirstlane(FULL[0]) >= k * CKP) break;
          __builtin_amdgcn_s_sleep(3);
        }
        const u32 now = (u32)__builtin_amdgcn_s_memrealtime();
        if (lane == bid) __builtin_amdgcn_raw_buffer_store_b64(u32x2{now, tag}, rp, k * 512 + lane * 8, 0, 16);
        const u32x2 v = __builtin_amdgcn_raw_buffer_load_b64(rp, (k - 1) * 512 + lane * 8, 0, PAUX);
        const bool ok = v[1] == tag;
        const float cnt = (float)__builtin_popcountll(__ballot(ok));
        const float sum = wave_sum(ok ? (float)(int)(v[0] - now) : 0.f);
        if (cnt >= 32.f) {
          const u32 mean = now + (int)(sum * __builtin_amdgcn_rcpf(cnt));
          if (k == 1) fixed = mean + SCH_A;
          const u32 cand = mean - ((((k - 1) * CKP + 18) * SCH_T) >> 8) - SCH_M;
          if (lane == 0) *(volatile LAS u32*)(L + L_T0) = (int)(cand - fixed) > 0 ? cand : fixed;
        }
      }
    }
    f32x4 base = {blo(xv[0]) + blo(bv[0]), bhi(xv[0]) + bhi(bv[0]), blo(xv[1]) + blo(bv[1]), bhi(xv[1]) + bhi(bv[1])};
    PIN(base);
    const rsrc_t rs = mkrsrc(ws + WS_Y + (size_t)(b * 32 + h) * 2048, 2048);
    STAMP(st2);
    lds_barrier();
    STAMP(st3);
    __builtin_amdgcn_s_sleep(YFIRST);
_Pragma("unroll 1")
    for (;; budget--) {
      __builtin_amdgcn_s_sleep(YSLEEP);
      asm volatile("" ::: "memory");
      g0 = bld<16>(rs, l16, 0); g1 = bld<16>(rs, l16 + 1024, 0);
      if (__all(fresh<YM>(g0, tag) && fresh<YM>(g1, tag)) || !budget) break;
    }
    float s[4];
#pragma unroll
    for (int i = 0; i < 4; i++) {
      float u = asf(g0[i] & ~YM) + asf(g1[i] & ~YM);
      u += dpp<ROR4>(u); u += dpp<ROR8>(u);
      s[i] = xrow_sum(u) + base[i];
    }
    if (lane < 4) *(u32x2*)(out + b * DM + 16 * h + 4 * lane) = u32x2{cvtpk(s[0], s[1]), cvtpk(s[2], s[3])};
    if (bid == 0 && lane == 0) __hip_atomic_store(ep, tag, RLX_AGENT);
    STAMP_OUT();
    __builtin_amdgcn_endpgm();
  }

  const int hw = w - 1 - NC;
  const bool has_mlp = h < MLPH;
  const int hm = has_mlp ? h : 0;
  if (lane == 0) ((volatile LAS u32*)(L + L_HF))[hw] = 0u;
  lds_barrier();
  PIN(tag); tag += 1u;
  f32x4 y[4];
  u32x4 wb[16];
  const u32 off2 = g * (8 * DM * 2) + c * 8;
  auto load_step = [&](rsrc_t r, int s, u32x4* dst) {
    u32x2 raw[8];
#pragma unroll
    for (int j = 0; j < 8; j++) raw[j] = __builtin_amdgcn_raw_buffer_load_b64(r, off2 + (j & 3) * (DM * 2), (32 * s + (j & 4)) * (DM * 2) + hw * 128, WAUX);
#pragma unroll
    for (int t = 0; t < 4; t++) {
#pragma unroll
      for (int j = 0; j < 4; j++) dst[t][j] = __builtin_amdgcn_perm(raw[2 * j + 1][t >> 1], raw[2 * j][t >> 1], (t & 1) ? 0x07060302u : 0x05040100u);
      PIN(dst[t]);
    }
  };
#define PAUSE(i) do { if (((i) + 1) % BURST == 0) asm volatile("s_waitcnt vmcnt(0)" ::: "memory"); } while (0)
  auto wait_slot = [&](int round) {
    const u32 due = T0 + (round * 8 + ((hw + (ROT ? b : 0)) & 7)) * SLOT;
_Pragma("unroll 1")
    for (; budget && (u32)__builtin_amdgcn_s_memrealtime() - tstart < due; budget--) __builtin_amdgcn_s_sleep(8);
  };
  {
    wait_slot(0);
    const rsrc_t rx = mkrsrc(x + b * DM, DM * 2);
    const u32x4 x8 = bld<0>(rx, l16, 0), xa = bld<0>(rx, (lane >> 3) * 16, hw * 128);
    const u32 b1r = b1[64 * hm + lane];
    const float b1v = asf(b1r << 16);
    const rsrc_t r1 = mkrsrc(w1 + hm * 64, NOSIDE || !has_mlp ? 0 : (DM * FF - hm * 64) * 2);
    const rsrc_t r2 = mkrsrc(w2 + (size_t)hm * 64 * DM, NOSIDE || !has_mlp ? 0 : 64 * DM * 2);
    const u32 off1 = (lane >> 3) * (8 * FF * 2) + (lane & 7) * 16;
#pragma unroll
    for (int i = 0; i < 8; i++) { wb[8 + i] = bld<WAUX>(r1, off1, (64 * hw + i) * (FF * 2)); PAUSE(i); }
    load_step(r2, 0, wb);
    load_step(r2, 1, wb + 4);
    float s = 0.f;
#pragma unroll
    for (int j = 0; j < 4; j++) s += blo(x8[j]) + bhi(x8[j]);
    const float mean = wave_sum(s) * (1.f / DM);
    float q = 0.f;
#pragma unroll
    for (int j = 0; j < 4; j++) { const float d0 = blo(x8[j]) - mean, d1 = bhi(x8[j]) - mean; q = fmaf(d0, d0, q); q = fmaf(d1, d1, q); }
    q = wave_sum(q);
    const float mb = rbf(mean), den = rbf(__builtin_amdgcn_sqrtf(rbf(rbf(q * (1.f / DM)) + 1e-5f))), r = __builtin_amdgcn_rcpf(den);
    f32x4 acc = {0.f, 0.f, 0.f, 0.f};
#pragma unroll
    for (int i = 0; i < 8; i++) acc = mfma(m8 & norm2((i & 1) ? bhi(xa[i >> 1]) : blo(xa[i >> 1]), mb, den, r), wb[8 + i], acc);
    if ((g >> 1) == (c >> 3)) *(LAS f32x4*)(L + L_HP + ((2 * hw + (g >> 1)) * 64 + 8 * (c & 7) + 4 * (g & 1)) * 4) = acc;
    LGKM0();
    if (lane == 0) ((volatile LAS u32*)(L + L_HF))[hw] = 1u;
_Pragma("unroll 1")
    for (; budget; budget--) {
      const u32x4 f0 = *(volatile LAS u32x4*)(L + L_HF), f1 = *(volatile LAS u32x4*)(L + L_HF + 16);
      if (__builtin_amdgcn_readfirstlane(f0[0] & f0[1] & f0[2] & f0[3] & f1[0] & f1[1] & f1[2] & f1[3])) break;
      __builtin_amdgcn_s_sleep(16);
    }
    asm volatile("" ::: "memory");
    float hs = 0.f;
#pragma unroll
    for (int i = 0; i < 2 * NHOLD; i++) hs += *(const LAS float*)(L + L_HP + (i * 64 + lane) * 4);
    const float hv = rbf(rbf(hs) + b1v);
    const float e = __builtin_amdgcn_exp2f((2.f * L2E * 0.7978845608028654f) * (hv + 0.044715f * hv * hv * hv));
    const float th = 1.f - 2.f / (e + 1.f);
    float gl = 0.5f * hv * (1.f + th);
    gl = has_mlp ? gl : 0.f;
    *(LAS u16*)(L + L_HV + hw * 128 + lane * 2) = (u16)cvtpk(gl, gl);
    LGKM0();
    const u32 row0 = c == 0 ? ~0u : 0u;
    const u32x4 h0 = *(const LAS u32x4*)(L + L_HV + hw * 128 + g * 16) & row0, h1 = *(const LAS u32x4*)(L + L_HV + hw * 128 + 64 + g * 16) & row0;
#pragma unroll
    for (int t = 0; t < 4; t++) {
      f32x4 a = {0.f, 0.f, 0.f, 0.f};
      a = mfma(h0, wb[t], a);
      y[t] = mfma(h1, wb[4 + t], a);
      PIN(y[t]);
    }
    wait_slot(1);
    const rsrc_t ro = mkrsrc(wo + (size_t)h * DH * DM, NOSIDE ? 0 : DH * DM * 2);
#pragma unroll
    for (int s = 0; s < 4; s++) load_step(ro, s, wb + 4 * s);
  }
  rsrc_t ry = mkrsrc(ws + WS_Y + (size_t)(b * 32 + 4 * hw) * 2048 + h * 64, 3 * 2048 + 64);
  u32 yoff = (c >> 2) * 2048 + (c & 3) * 16;
  asm volatile("" : "+s"(ry), "+v"(yoff), "+v"(tag));
  STAMP(st2);
  const LAS unsigned char* ap = L + L_OP + (c & 7) * OPS_ + g * 16;
  lds_barrier();
  {
    u32x4 a[4];
#pragma unroll
    for (int s = 0; s < 4; s++) a[s] = *(const LAS u32x4*)(ap + s * 64);
#pragma unroll
    for (int s = 0; s < 4; s++)
#pragma unroll
      for (int t = 0; t < 4; t++) y[t] = mfma(a[s], wb[4 * s + t], y[t]);
    f32x4 s;
#pragma unroll
    for (int t = 0; t < 4; t++) {
      const float u = (y[t][0] + y[t][1]) + (y[t][2] + y[t][3]);
      auto p = __builtin_amdgcn_permlane16_swap(asu(u), asu(u), false, false);
      s[t] = asf(p[0]) + asf(p[1]);
    }
    if (g == 0) __builtin_amdgcn_raw_buffer_store_b128(tagged<YM>(s, tag), ry, yoff, 0, 16);
  }
  STAMP(st3);
  STAMP_OUT();
  __builtin_amdgcn_endpgm();
}

extern "C" void kernel_launch(void* const* d_in, const int* in_sizes, int n_in, void* d_out, int out_size, void* d_ws,
                              size_t ws_size, hipStream_t stream) {
  fused_decode<<<NB * NH, NW * 64, 0, stream>>>((const u16*)d_in[0], (const u16*)d_in[1], (const u16*)d_in[2], (const u16*)d_in[3],
                                                (const u16*)d_in[4], (const u16*)d_in[6], (const u16*)d_in[7], (const u16*)d_in[8],
                                                (const u16*)d_in[9], (u16*)d_out, (unsigned char*)d_ws);
}
```

```cpp
#include <hip/hip_runtime.h>
#include <cstdint>

typedef unsigned short u16;
typedef unsigned u32;
typedef unsigned long long u64;
typedef u32 u32x4 __attribute__((ext_vector_type(4)));
typedef u32 u32x2 __attribute__((ext_vector_type(2)));
typedef float f32x4 __attribute__((ext_vector_type(4)));
typedef short bf16x8 __attribute__((ext_vector_type(8)));
typedef __bf16 bf16x2 __attribute__((ext_vector_type(2)));
typedef __amdgpu_buffer_rsrc_t rsrc_t;
#define GAS __attribute__((address_space(1)))
#define LAS __attribute__((address_space(3)))
#define RLX_AGENT __ATOMIC_RELAXED, __HIP_MEMORY_SCOPE_AGENT

#define WIN 20
#define WIN0 WIN
#define WINK WIN
#define WINT WIN
#define TAILK 128
#define HEADK 192
#define WARM 0
#define QSLEEP 8
#define T0 800
#define SLOT 300
#define BURST 4
#define ROT 1
#define WA 18
#define WB 20
#define WC 22
#define WD 26
#define WE 30
#define SCH_A 10
#define SCH_T 897
#define LB 40
#define LC 80
#define LD 130
#define DRY 0
#define YSLEEP 0
#define NOQ 0
#define VSLEEP 1
#define CODEKB 0
#define PEER 1
#define SCH_M 40
#define CKP 256
#define PAUX 16
#define WARMB 0
#define YPIPE 0
#define YFIRST 0
#define YGAP 6
#define CKEND 200
#define HEARLY 0
#define YTOUCH 0
#define YAUXS 16
#define YAUXL 16
#define GRP 2
#define WAUX 0
#define NOSIDE 0
#define HSLEEP 40
#define SN1 144
#define SN2 528
#define SN3 V0
#define STAMPS 0

constexpr int NB = 8, DM = 512, NH = 32, SK = 4096, DH = 128, FF = 1792;
constexpr int NC = 4, NHOLD = 8, NW = 1 + NC + NHOLD + 1;
constexpr int MLPH = FF / 64;
constexpr int RING = 128;
constexpr int K0 = 16, V0 = K0 + 1024, END = V0 + 1024;
constexpr size_t WS_Y = 65536, WS_Q = WS_Y + 8 * 32 * 2048, WS_P = 4096, WS_STAMP = 8u << 20;
constexpr u32 QM = 0x3fu, YM = 0xffu;
constexpr u32 SPIN_CAP = 1u << 18;
constexpr int OPS_ = DH * 2 + 16;
constexpr int L_RING = 0, L_SC = RING * 1024, L_HP = L_SC + SK * 4, L_OP = L_HP + 2 * NHOLD * 64 * 4, L_HV = L_OP + 2 * NC * OPS_, L_Q = L_HV + NHOLD * 128;
constexpr int L_FULL = L_Q + 256, L_NEXT = L_FULL + 256, L_QRDY = L_NEXT + 16, L_ML = L_QRDY + 16, L_MF = L_ML + 32, L_PF = L_MF + 16, L_HF = L_PF + 16, L_T0 = L_HF + 32, L_END = L_T0 + 16;

__device__ __forceinline__ float asf(u32 v) { return __builtin_bit_cast(float, v); }
__device__ __forceinline__ u32 asu(float v) { return __builtin_bit_cast(u32, v); }
__device__ __forceinline__ float blo(u32 v) { return asf(v << 16); }
__device__ __forceinline__ float bhi(u32 v) { return asf(v & 0xffff0000u); }
__device__ __forceinline__ u32 cvtpk(float lo, float hi) {
  bf16x2 r = {(__bf16)lo, (__bf16)hi};
  return __builtin_bit_cast(u32, r);
}
__device__ __forceinline__ float rbf(float x) { return (float)(__bf16)x; }
__device__ __forceinline__ float dot2(u32 a, u32 b, float c) {
  return __builtin_amdgcn_fdot2_f32_bf16(__builtin_bit_cast(bf16x2, a), __builtin_bit_cast(bf16x2, b), c, false);
}
template <int CTRL> __device__ __forceinline__ float dpp(float x) {
  return asf(__builtin_amdgcn_mov_dpp(asu(x), CTRL, 0xf, 0xf, true));
}
template <int CTRL> __device__ __forceinline__ u32 dppu(u32 x) { return __builtin_amdgcn_mov_dpp(x, CTRL, 0xf, 0xf, true); }
constexpr int XOR1 = 0xB1, XOR2 = 0x4E, HMIR = 0x141, ROR8 = 0x128, ROR4 = 0x124;
__device__ __forceinline__ float xrow_max(float x) {
  auto s = __builtin_amdgcn_permlane16_swap(asu(x), asu(x), false, false);
  x = fmaxf(asf(s[0]), asf(s[1]));
  auto t = __builtin_amdgcn_permlane32_swap(asu(x), asu(x), false, false);
  return fmaxf(asf(t[0]), asf(t[1]));
}
__device__ __forceinline__ float xrow_sum(float x) {
  auto s = __builtin_amdgcn_permlane16_swap(asu(x), asu(x), false, false);
  x = asf(s[0]) + asf(s[1]);
  auto t = __builtin_amdgcn_permlane32_swap(asu(x), asu(x), false, false);
  return asf(t[0]) + asf(t[1]);
}
__device__ __forceinline__ float half_sum(float x) {
  x += dpp<XOR1>(x); x += dpp<XOR2>(x); x += dpp<HMIR>(x); return xrow_sum(x);
}
__device__ __forceinline__ float wave_sum(float x) { x = half_sum(x); return x + dpp<ROR8>(x); }
__device__ __forceinline__ float wave_max(float x) {
  x = fmaxf(x, dpp<XOR1>(x)); x = fmaxf(x, dpp<XOR2>(x)); x = fmaxf(x, dpp<HMIR>(x)); x = fmaxf(x, dpp<ROR8>(x)); return xrow_max(x);
}
__device__ __forceinline__ f32x4 mfma(u32x4 a, u32x4 b, f32x4 c) {
  return __builtin_amdgcn_mfma_f32_16x16x32_bf16(__builtin_bit_cast(bf16x8, a), __builtin_bit_cast(bf16x8, b), c, 0, 0, 0);
}
__device__ __forceinline__ rsrc_t mkrsrc(const void* p, u32 bytes) {
  return __builtin_amdgcn_make_buffer_rsrc((void*)p, 0, bytes, 0x00020000);
}
template <int AUX> __device__ __forceinline__ u32x4 bld(rsrc_t r, u32 voff, u32 soff) {
  return __builtin_amdgcn_raw_buffer_load_b128(r, voff, soff, AUX);
}
#define SB() __builtin_amdgcn_sched_barrier(0)
#define PIN(x) asm volatile("" : "+v"(x))
#define LGKM0() asm volatile("s_waitcnt lgkmcnt(0)" ::: "memory")
__device__ __forceinline__ void lds_barrier() { asm volatile("s_waitcnt lgkmcnt(0)\n\ts_barrier" ::: "memory"); }
template <int N> struct IC { static constexpr int v = N; };
template <u32 M> __device__ __forceinline__ u32x4 tagged(f32x4 v, u32 tag) {
  u32x4 r;
#pragma unroll
  for (int i = 0; i < 4; i++) r[i] = ((asu(v[i]) + (M + 1) / 2) & ~M) | (tag & M);
  return r;
}
template <u32 M> __device__ __forceinline__ bool fresh(u32x4 v, u32 tag) {
  return (((v[0] ^ tag) | (v[1] ^ tag) | (v[2] ^ tag) | (v[3] ^ tag)) & M) == 0u;
}
__device__ __forceinline__ u32 norm2(float xv, float mb, float den, float r) {
  const float a = rbf(xv - mb), q0 = a * r, qq = fmaf(fmaf(-q0, den, a), r, q0);
  return cvtpk(qq, qq);
}

__global__ __launch_bounds__(NW * 64, 4) void fused_decode(
    const u16* __restrict__ x, const u16* __restrict__ Kc, const u16* __restrict__ Vc, const u16* __restrict__ wq,
    const u16* __restrict__ wo, const u16* __restrict__ w1, const u16* __restrict__ b1, const u16* __restrict__ w2,
    const u16* __restrict__ b2, u16* __restrict__ out, unsigned char* __restrict__ ws) {
  __shared__ __attribute__((aligned(16))) unsigned char lds_raw[L_END];
  LAS unsigned char* L = (LAS unsigned char*)lds_raw;
#define STAMP(s)
#define STAMP_OUT()
  const u32 tstart = (u32)__builtin_amdgcn_s_memrealtime();
  const int tid = threadIdx.x, lane = tid & 63, w = __builtin_amdgcn_readfirstlane(tid >> 6);
  const int g = lane >> 4, c = lane & 15;
  const int bid = blockIdx.x, h = (bid & 7) * 4 + ((bid >> 3) & 3), b = bid >> 5;
  const u32 l16 = lane * 16;
  u32 budget = SPIN_CAP;
  volatile LAS u32* FULL = (volatile LAS u32*)(L + L_FULL);
  volatile LAS u32* NEXT = (volatile LAS u32*)(L + L_NEXT);

  if (__builtin_expect(w == 0, 1)) {
    __builtin_amdgcn_s_setprio(3);
    const size_t bh = (size_t)(b * NH + h) * (SK * DH);
    const int qc = h * DH + 64 * (b & 1);
    const rsrc_t rq = mkrsrc(wq + (size_t)(128 * (b >> 1)) * (NH * DH) + qc, (128 * NH * DH - qc) * 2);
    const rsrc_t rk = mkrsrc(Kc + bh, SK * DH * 2);
    const u32 ring0 = (u32)(uintptr_t)L;
    const u32 qoff = (lane >> 3) * (16 * NH * DH * 2) + (lane & 7) * 16;
#pragma unroll
    for (int i = 0; i < 16; i++)
      asm volatile("s_mov_b32 m0, %0\n\ts_nop 0\n\tbuffer_load_dwordx4 %1, %2, %3 offen nt lds"
                   :: "s"(ring0 + i * 1024), "v"(qoff), "s"(rq), "s"(i * (NH * DH * 2)) : "memory");
    const u32 fulladdr = ring0 + L_FULL + lane * 4;
    FULL[lane] = 0u;
#define DMA(off) "buffer_load_dwordx4 %0, %1, %2 offen offset:" #off " nt lds\n\t"
    auto quad = [&](rsrc_t r, u32 m0v, u32 soff, auto WINDOW, u32 pos) {
      constexpr int win = decltype(WINDOW)::v;
      constexpr bool wait = win != 0;
      constexpr int W = wait ? win - GRP : 0;
      constexpr bool pub0 = W % 4 == 0;
      static_assert(win % GRP == 0 && win % 2 == 0 && win <= 34 && (GRP == 2 || GRP == 4), "");
      const u32 pub = pos - (pub0 ? W : W - 2);
#define M0 "s_mov_b32 m0, %3\n\ts_nop 0\n\t"
#define WT "s_waitcnt vmcnt(%6)\n\t"
#define PB "ds_max_u32 %4, %5\n\t"
#define OPS :: "v"(l16), "s"(r), "s"(soff), "s"(m0v), "v"(fulladdr), "v"(pub), "i"(W) : "memory"
      if constexpr (!wait) asm volatile(M0 DMA(0) DMA(1024) DMA(2048) DMA(3072) OPS);
      else if constexpr (GRP == 4) asm volatile(M0 WT DMA(0) DMA(1024) DMA(2048) DMA(3072) PB OPS);
      else if constexpr (pub0) asm volatile(M0 WT DMA(0) DMA(1024) PB WT DMA(2048) DMA(3072) OPS);
      else asm volatile(M0 WT DMA(0) DMA(1024) WT DMA(2048) DMA(3072) PB OPS);
    };
#pragma unroll
    for (int j = 0; j < 16; j += 4) quad(rk, ring0 + (K0 + j) * 1024, j * 1024, IC<0>{}, 0);
    lds_barrier();
    u32 limit = RING;
    int late = WARMB ? LD : -1;
    {
#pragma unroll 1
      for (u32 n = K0 + 16; n < END; n += 16) {
        const u32 j = (n - K0) & 1023;
        const rsrc_t r = mkrsrc((n < V0 ? Kc : Vc) + bh, SK * DH * 2);
        if (__builtin_expect(n + 16 > limit, 0)) {
_Pragma("unroll 1")
          for (u32 spins = 0; budget; spins++, budget--) {
            const u32x4 nx = *(volatile LAS u32x4*)NEXT;
            limit = __builtin_amdgcn_readfirstlane(min(min(nx[0], nx[1]), min(nx[2], nx[3]))) + RING;
            if (n + 16 <= limit) break;
            if (spins == 0) { asm volatile("s_waitcnt vmcnt(0)" ::: "memory"); FULL[lane] = n; }
            __builtin_amdgcn_s_sleep(1);
          }
        }
        const u32 m0v = ring0 + (n & (RING - 1)) * 1024;
        u64 now = __builtin_amdgcn_s_memrealtime();
        u32 t0v = *(volatile LAS u32*)(L + L_T0);
        auto body = [&](auto WINDOW) {
#pragma unroll
          for (int k = 0; k < 16; k += 4) quad(r, m0v + k * 1024, (j + k) * 1024, WINDOW, n + k);
        };
        if (late < 0) body(IC<WA>{});
        else if (late < LB) body(IC<WB>{});
        else if (late < LC) body(IC<WC>{});
        else if (late < LD) body(IC<WD>{});
        else body(IC<WE>{});
        asm volatile("" : "+s"(now));
        PIN(t0v);
        late = (int)((u32)now - __builtin_amdgcn_readfirstlane(t0v) - ((n * SCH_T) >> 8));
        if (WARMB && n < K0 + 80) late = n == K0 + 16 ? LC : n == K0 + 32 ? LB : n == K0 + 48 ? -1 : 0;
      }
    }
#pragma unroll
    for (int k = (WE - GRP) / 4 * 4; k >= 0; k -= 4)
      asm volatile("s_waitcnt vmcnt(%2)\n\tds_max_u32 %0, %1" :: "v"(fulladdr), "v"(END - k), "i"(k) : "memory");
    LGKM0();
    STAMP_OUT();
    __builtin_amdgcn_endpgm();
  }

  GAS u32* ep = (GAS u32*)ws;
  u32 tag = __hip_atomic_load(ep, RLX_AGENT);
  u32x4 m4, m8;
#pragma unroll
  for (int r = 0; r < 4; r++) {
    const u32 mk = (((c & 7) >> 1) == r) ? ((c & 1) ? 0xffff0000u : 0x0000ffffu) : 0u;
    m8[r] = mk; m4[r] = c < 8 ? mk : 0u;
  }
  auto wait_full = [&](u32 need) {
_Pragma("unroll 1")
    for (; budget; budget--) {
      if (__builtin_amdgcn_readfirstlane(FULL[0]) >= need) break;
      __builtin_amdgcn_s_sleep(1);
    }
    asm volatile("" ::: "memory");
  };
  constexpr float SCALE = 0.08838834764831845f, L2E = 1.4426950408889634f;

  if (w <= NC) {
    const int cw = w - 1;
    const rsrc_t rx = mkrsrc(x + (2 * cw) * DM, 2 * DM * 2);
    const u32 xb = (c >> 3) * (DM * 2);
    u32x4 s0 = bld<0>(rx, xb + (g * 8 + (c & 7)) * 32, 0), s1 = bld<0>(rx, xb + (g * 8 + (c & 7)) * 32 + 16, 0);
    u32x2 xq = __builtin_amdgcn_raw_buffer_load_b64(rx, xb + (g * 8 + (c & 7)) * 8, (b >> 1) * 256, 0);
    if (lane == 0) { NEXT[cw] = 0u; ((volatile LAS u32*)(L + L_MF))[cw] = 0u; ((volatile LAS u32*)(L + L_PF))[cw] = 0u; }
    lds_barrier();
    PIN(tag); PIN(s0); PIN(s1); PIN(xq);
    tag += 1u;
    u32x4 t[16];
    {
      float s = 0.f;
#pragma unroll
      for (int j = 0; j < 4; j++) s += (blo(s0[j]) + bhi(s0[j])) + (blo(s1[j]) + bhi(s1[j]));
      const float mean = half_sum(s) * (1.f / DM);
      float q = 0.f;
#pragma unroll
      for (int j = 0; j < 4; j++) {
        const float d0 = blo(s0[j]) - mean, d1 = bhi(s0[j]) - mean, d2 = blo(s1[j]) - mean, d3 = bhi(s1[j]) - mean;
        q = fmaf(d0, d0, q); q = fmaf(d1, d1, q); q = fmaf(d2, d2, q); q = fmaf(d3, d3, q);
      }
      q = half_sum(q);
      const float mb = rbf(mean), den = rbf(__builtin_amdgcn_sqrtf(rbf(rbf(q * (1.f / DM)) + 1e-5f))), r = __builtin_amdgcn_rcpf(den);
      LAS unsigned char* xl = L + L_SC + cw * 1024 + (c >> 3) * 512;
      *(LAS u32x4*)(xl + (g * 8 + (c & 7)) * 16) =
          u32x4{norm2(blo(xq[0]), mb, den, r), norm2(bhi(xq[0]), mb, den, r), norm2(blo(xq[1]), mb, den, r), norm2(bhi(xq[1]), mb, den, r)};
      LGKM0();
      wait_full(K0);
      f32x4 a0 = {0.f, 0.f, 0.f, 0.f}, a1 = a0;
#pragma unroll 1
      for (int i = 0; i < 4; i++) {
        const u32x4 e0 = *(const LAS u32x4*)(xl + g * 128 + i * 16), e1 = *(const LAS u32x4*)(xl + g * 128 + 64 + i * 16);
#pragma unroll
        for (int k = 0; k < 4; k++) t[k] = *(const LAS u32x4*)(L + (4 * i + k) * 1024 + l16);
#pragma unroll
        for (int k = 0; k < 4; k++) { a0 = mfma(m8 & e0[k], t[k], a0); a1 = mfma(m8 & e1[k], t[k], a1); }
      }
      LGKM0();
      NEXT[cw] = K0 + 16 * cw;
      f32x4 z;
#pragma unroll
      for (int i = 0; i < 4; i++) { const float u = c < 8 ? a0[i] : a1[i]; z[i] = u + dpp<ROR8>(u); }
      const rsrc_t rqs = mkrsrc(ws + WS_Q + (size_t)(h * 8 + 2 * cw) * 2048 + ((b & 1) * 4 + (b >> 1)) * 256, (NOQ & 1) ? 0 : 2048 + 256);
      if (c < 8) __builtin_amdgcn_raw_buffer_store_b128(tagged<QM>(z, tag), rqs, (g >> 1) * 2048 + (2 * c + (g & 1)) * 16, 0, 16);
    }
    STAMP(st1);
    u32x4 qp;
    float mx = -INFINITY;
#pragma unroll 1
    for (int i = 0; i < 16; i++) {
      const u32 n = K0 + 16 * (cw + NC * i);
      wait_full(n + 16);
      const LAS unsigned char* src = L + (n & (RING - 1)) * 1024 + l16;
#pragma unroll
      for (int k = 0; k < 16; k++) t[k] = *(const LAS u32x4*)(src + k * 1024);
      LGKM0();
      NEXT[cw] = i < 15 ? n + 16 * NC : V0 + 4 * cw;
      if (i == 0) {
_Pragma("unroll 1")
        for (; budget; budget--) {
          if (__builtin_amdgcn_readfirstlane(*(volatile LAS u32*)(L + L_QRDY))) break;
          __builtin_amdgcn_s_sleep(1);
        }
        asm volatile("" ::: "memory");
        qp = *(const LAS u32x4*)(L + L_Q + c * 16);
        STAMP(st2);
      }
      float s[16];
#pragma unroll
      for (int k = 0; k < 16; k++) {
        PIN(t[k]);
        float d = dot2(t[k][0], qp[0], 0.f);
        d = dot2(t[k][1], qp[1], d); d = dot2(t[k][2], qp[2], d); s[k] = dot2(t[k][3], qp[3], d);
      }
#define TR_STEP(HALF, CTRL, BIT) _Pragma("unroll") for (int k = 0; k < (HALF); k++) { \
        const float lo_ = s[k], hi_ = s[k + (HALF)]; const bool up_ = (c & (BIT)) != 0; \
        s[k] = (up_ ? hi_ : lo_) + dpp<CTRL>(up_ ? lo_ : hi_); }
      TR_STEP(8, ROR8, 8) TR_STEP(4, HMIR, 4) TR_STEP(2, XOR2, 2) TR_STEP(1, XOR1, 1)
#undef TR_STEP
      const float sc = rbf(rbf(s[0]) * SCALE);
      mx = fmaxf(mx, sc);
      *(LAS float*)(L + L_SC + ((cw + NC * i) * 64 + lane) * 4) = sc;
    }
    {
      mx = wave_max(mx);
      float e[16], ls = 0.f;
#pragma unroll
      for (int i = 0; i < 16; i++) {
        e[i] = __builtin_amdgcn_exp2f((*(const LAS float*)(L + L_SC + ((cw + NC * i) * 64 + lane) * 4) - mx) * L2E);
        ls += e[i];
      }
      ls = wave_sum(ls);
      if (lane == 0) { ((volatile LAS float*)(L + L_ML))[cw] = mx; ((volatile LAS float*)(L + L_ML))[4 + cw] = ls; }
      LGKM0();
      if (lane == 0) ((volatile LAS u32*)(L + L_MF))[cw] = 1u;
_Pragma("unroll 1")
      for (; budget; budget--) {
        const u32x4 f = *(volatile LAS u32x4*)(L + L_MF);
        if (__builtin_amdgcn_readfirstlane(f[0] & f[1] & f[2] & f[3])) break;
        __builtin_amdgcn_s_sleep(1);
      }
      asm volatile("" ::: "memory");
      const f32x4 ma = *(volatile LAS f32x4*)(L + L_ML), la = *(volatile LAS f32x4*)(L + L_ML + 16);
      const float M = fmaxf(fmaxf(ma[0], ma[1]), fmaxf(ma[2], ma[3]));
      float Ls = 0.f;
#pragma unroll
      for (int i = 0; i < 4; i++) Ls = fmaf(__builtin_amdgcn_exp2f((ma[i] - M) * L2E), la[i], Ls);
      const float f = __builtin_amdgcn_exp2f((mx - M) * L2E) / Ls;
#pragma unroll
      for (int i = 0; i < 16; i++) { const float p = e[i] * f; *(LAS u32*)(L + L_SC + ((cw + NC * i) * 64 + lane) * 4) = cvtpk(p, p); }
      LGKM0();
      if (lane == 0) ((volatile LAS u32*)(L + L_PF))[cw] = 1u;
_Pragma("unroll 1")
      for (; budget; budget--) {
        const u32x4 f = *(volatile LAS u32x4*)(L + L_PF);
        if (__builtin_amdgcn_readfirstlane(f[0] & f[1] & f[2] & f[3])) break;
        __builtin_amdgcn_s_sleep(1);
      }
      asm volatile("" ::: "memory");
    }
    STAMP(st3);
    f32x4 oacc = {0.f, 0.f, 0.f, 0.f};
#pragma unroll 1
    for (int i = 0; i < 64; i++) {
      const u32 n = V0 + 16 * i + 4 * cw;
      u32x4 pp = *(const LAS u32x4*)(L + L_SC + (i * 64 + g * 16 + 4 * cw) * 4);
      u32x4 pa[4];
#pragma unroll
      for (int k = 0; k < 4; k++) { pa[k] = m4 & pp[k]; PIN(pa[k]); }
      const LAS unsigned char* src = L + (n & (RING - 1)) * 1024 + l16;
_Pragma("unroll 1")
      for (; budget; budget--) {
        if (__builtin_amdgcn_readfirstlane(FULL[0]) >= n + 4) break;
        __builtin_amdgcn_s_sleep(VSLEEP);
      }
      asm volatile("" ::: "memory");
#pragma unroll
      for (int k = 0; k < 4; k++) t[k] = *(const LAS u32x4*)(src + k * 1024);
#pragma unroll
      for (int k = 0; k < 4; k++) oacc = mfma(pa[k], t[k], oacc);
      PIN(oacc);
      LGKM0();
      NEXT[cw] = n + 16;
    }
    {
      const u32x2 hi = {cvtpk(oacc[0], oacc[1]), cvtpk(oacc[2], oacc[3])};
      const u32x2 lo = {cvtpk(oacc[0] - blo(hi[0]), oacc[1] - bhi(hi[0])), cvtpk(oacc[2] - blo(hi[1]), oacc[3] - bhi(hi[1]))};
      LAS unsigned char* op = L + L_OP + 2 * cw * OPS_ + (8 * c + 4 * g) * 2;
      if (lane < 32) { *(LAS u32x2*)op = hi; *(LAS u32x2*)(op + OPS_) = lo; }
    }
    lds_barrier();
    STAMP_OUT();
    __builtin_amdgcn_endpgm();
  }

  if (w == NW - 1) {
    u32x2 xv = *(const u32x2*)(x + b * DM + 16 * h + 4 * (lane & 3)), bv = *(const u32x2*)(b2 + 16 * h + 4 * (lane & 3));
    if (lane == 0) { *(volatile LAS u32*)(L + L_QRDY) = 0u; *(volatile LAS u32*)(L + L_T0) = tstart + SCH_A; }
    lds_barrier();
    PIN(tag); PIN(xv); PIN(bv); tag += 1u;
    const rsrc_t rp = mkrsrc(ws + WS_P, (END / CKP + 1) * 512);
    if (PEER && lane == bid) __builtin_amdgcn_raw_buffer_store_b64(u32x2{tstart, tag}, rp, lane * 8, 0, 16);
    u32x4 g0, g1;
    {
      const rsrc_t rqs = mkrsrc(ws + WS_Q + (size_t)(h * 8 + b) * 2048, (NOQ & 2) ? 0 : 2048);
_Pragma("unroll 1")
      for (; budget; budget--) {
        const u32x4 nx = *(volatile LAS u32x4*)NEXT;
        if (__builtin_amdgcn_readfirstlane(min(min(nx[0], nx[1]), min(nx[2], nx[3])))) break;
        __builtin_amdgcn_s_sleep(2);
      }
_Pragma("unroll 1")
      for (;; budget--) {
        __builtin_amdgcn_s_sleep(QSLEEP);
        asm volatile("" ::: "memory");
        g0 = bld<16>(rqs, l16, 0); g1 = bld<16>(rqs, l16 + 1024, 0);
        if (__all(fresh<QM>(g0, tag) && fresh<QM>(g1, tag)) || !budget || NOQ) break;
      }
      float qa[4], qb[4];
#pragma unroll
      for (int i = 0; i < 4; i++) { qa[i] = xrow_sum(asf(g0[i] & ~QM)); qb[i] = xrow_sum(asf(g1[i] & ~QM)); }
      if (g == 0) *(LAS u32x2*)(L + L_Q + c * 8) = u32x2{cvtpk(qa[0], qa[1]), cvtpk(qa[2], qa[3])};
      if (g == 1) *(LAS u32x2*)(L + L_Q + 128 + c * 8) = u32x2{cvtpk(qb[0], qb[1]), cvtpk(qb[2], qb[3])};
      LGKM0();
      if (lane == 0) *(volatile LAS u32*)(L + L_QRDY) = 1u;
      STAMP(st1);
    }
    {
      u32 fixed = tstart + SCH_A;
_Pragma("unroll 1")
      for (u32 k = 1; k * CKP < END - CKEND; k++) {
_Pragma("unroll 1")
        for (; budget; budget--) {
          if (__builtin_amdgcn_readfirstlane(FULL[0]) >= k * CKP) break;
          __builtin_amdgcn_s_sleep(3);
        }
        const u32 now = (u32)__builtin_amdgcn_s_memrealtime();
        if (lane == bid) __builtin_amdgcn_raw_buffer_store_b64(u32x2{now, tag}, rp, k * 512 + lane * 8, 0, 16);
        const u32x2 v = __builtin_amdgcn_raw_buffer_load_b64(rp, (k - 1) * 512 + lane * 8, 0, PAUX);
        const bool ok = v[1] == tag;
        const float cnt = (float)__builtin_popcountll(__ballot(ok));
        const float sum = wave_sum(ok ? (float)(int)(v[0] - now) : 0.f);
        if (cnt >= 32.f) {
          const u32 mean = now + (int)(sum * __builtin_amdgcn_rcpf(cnt));
          if (k == 1) fixed = mean + SCH_A;
          const u32 cand = mean - ((((k - 1) * CKP + 18) * SCH_T) >> 8) - SCH_M;
          if (lane == 0) *(volatile LAS u32*)(L + L_T0) = (int)(cand - fixed) > 0 ? cand : fixed;
        }
      }
    }
    f32x4 base = {blo(xv[0]) + blo(bv[0]), bhi(xv[0]) + bhi(bv[0]), blo(xv[1]) + blo(bv[1]), bhi(xv[1]) + bhi(bv[1])};
    PIN(base);
    const rsrc_t rs = mkrsrc(ws + WS_Y + (size_t)(b * 32 + h) * 2048, 2048);
    STAMP(st2);
    asm volatile("s_waitcnt vmcnt(0)" ::: "memory");
    lds_barrier();
    STAMP(st3);
    __builtin_amdgcn_s_sleep(YFIRST);
_Pragma("unroll 1")
    for (;; budget--) {
      asm volatile("" ::: "memory");
      __builtin_amdgcn_s_sleep(YSLEEP);
      g0 = bld<YAUXL>(rs, l16, 0); g1 = bld<YAUXL>(rs, l16 + 1024, 0);
      if (__all(fresh<YM>(g0, tag) & fresh<YM>(g1, tag)) || !budget) break;
    }
    float s[4];
#pragma unroll
    for (int i = 0; i < 4; i++) {
      float u = asf(g0[i] & ~YM) + asf(g1[i] & ~YM);
      u += dpp<ROR4>(u); u += dpp<ROR8>(u);
      s[i] = xrow_sum(u) + base[i];
    }
    if (lane < 4) *(u32x2*)(out + b * DM + 16 * h + 4 * lane) = u32x2{cvtpk(s[0], s[1]), cvtpk(s[2], s[3])};
    if (bid == 0 && lane == 0) __hip_atomic_store(ep, tag, RLX_AGENT);
    STAMP_OUT();
    asm volatile("s_waitcnt vmcnt(0)" ::: "memory");
    __builtin_amdgcn_endpgm();
  }

  const int hw = w - 1 - NC;
  const bool has_mlp = h < MLPH;
  const int hm = has_mlp ? h : 0;
  if (lane == 0) ((volatile LAS u32*)(L + L_HF))[hw] = 0u;
  lds_barrier();
  PIN(tag); tag += 1u;
  f32x4 y[4];
  u32x4 wb[16];
  const u32 off2 = g * (8 * DM * 2) + c * 8;
  auto load_step = [&](rsrc_t r, int s, u32x4* dst) {
    u32x2 raw[8];
#pragma unroll
    for (int j = 0; j < 8; j++) raw[j] = __builtin_amdgcn_raw_buffer_load_b64(r, off2 + (j & 3) * (DM * 2), (32 * s + (j & 4)) * (DM * 2) + hw * 128, WAUX);
#pragma unroll
    for (int t = 0; t < 4; t++) {
#pragma unroll
      for (int j = 0; j < 4; j++) dst[t][j] = __builtin_amdgcn_perm(raw[2 * j + 1][t >> 1], raw[2 * j][t >> 1], (t & 1) ? 0x07060302u : 0x05040100u);
      PIN(dst[t]);
    }
  };
#define PAUSE(i) do { if (((i) + 1) % BURST == 0) asm volatile("s_waitcnt vmcnt(0)" ::: "memory"); } while (0)
  auto wait_slot = [&](int round) {
    const u32 due = T0 + (round * 8 + ((hw + (ROT ? b : 0)) & 7)) * SLOT;
_Pragma("unroll 1")
    for (; budget && (u32)__builtin_amdgcn_s_memrealtime() - tstart < due; budget--) __builtin_amdgcn_s_sleep(8);
  };
  {
    wait_slot(0);
    const rsrc_t rx = mkrsrc(x + b * DM, DM * 2);
    const u32x4 x8 = bld<0>(rx, l16, 0), xa = bld<0>(rx, (lane >> 3) * 16, hw * 128);
    const u32 b1r = b1[64 * hm + lane];
    const float b1v = asf(b1r << 16);
    const rsrc_t r1 = mkrsrc(w1 + hm * 64, NOSIDE || !has_mlp ? 0 : (DM * FF - hm * 64) * 2);
    const rsrc_t r2 = mkrsrc(w2 + (size_t)hm * 64 * DM, NOSIDE || !has_mlp ? 0 : 64 * DM * 2);
    const u32 off1 = (lane >> 3) * (8 * FF * 2) + (lane & 7) * 16;
#pragma unroll
    for (int i = 0; i < 8; i++) { wb[8 + i] = bld<WAUX>(r1, off1, (64 * hw + i) * (FF * 2)); PAUSE(i); }
    load_step(r2, 0, wb);
    load_step(r2, 1, wb + 4);
    float s = 0.f;
#pragma unroll
    for (int j = 0; j < 4; j++) s += blo(x8[j]) + bhi(x8[j]);
    const float mean = wave_sum(s) * (1.f / DM);
    float q = 0.f;
#pragma unroll
    for (int j = 0; j < 4; j++) { const float d0 = blo(x8[j]) - mean, d1 = bhi(x8[j]) - mean; q = fmaf(d0, d0, q); q = fmaf(d1, d1, q); }
    q = wave_sum(q);
    const float mb = rbf(mean), den = rbf(__builtin_amdgcn_sqrtf(rbf(rbf(q * (1.f / DM)) + 1e-5f))), r = __builtin_amdgcn_rcpf(den);
    f32x4 acc = {0.f, 0.f, 0.f, 0.f};
#pragma unroll
    for (int i = 0; i < 8; i++) acc = mfma(m8 & norm2((i & 1) ? bhi(xa[i >> 1]) : blo(xa[i >> 1]), mb, den, r), wb[8 + i], acc);
    if ((g >> 1) == (c >> 3)) *(LAS f32x4*)(L + L_HP + ((2 * hw + (g >> 1)) * 64 + 8 * (c & 7) + 4 * (g & 1)) * 4) = acc;
    LGKM0();
    if (lane == 0) ((volatile LAS u32*)(L + L_HF))[hw] = 1u;
_Pragma("unroll 1")
    for (; budget; budget--) {
      const u32x4 f0 = *(volatile LAS u32x4*)(L + L_HF), f1 = *(volatile LAS u32x4*)(L + L_HF + 16);
      if (__builtin_amdgcn_readfirstlane(f0[0] & f0[1] & f0[2] & f0[3] & f1[0] & f1[1] & f1[2] & f1[3])) break;
      __builtin_amdgcn_s_sleep(16);
    }
    asm volatile("" ::: "memory");
    float hs = 0.f;
#pragma unroll
    for (int i = 0; i < 2 * NHOLD; i++) hs += *(const LAS float*)(L + L_HP + (i * 64 + lane) * 4);
    const float hv = rbf(rbf(hs) + b1v);
    const float e = __builtin_amdgcn_exp2f((2.f * L2E * 0.7978845608028654f) * (hv + 0.044715f * hv * hv * hv));
    const float th = 1.f - 2.f / (e + 1.f);
    float gl = 0.5f * hv * (1.f + th);
    gl = has_mlp ? gl : 0.f;
    *(LAS u16*)(L + L_HV + hw * 128 + lane * 2) = (u16)cvtpk(gl, gl);
    LGKM0();
    const u32 row0 = c == 0 ? ~0u : 0u;
    const u32x4 h0 = *(const LAS u32x4*)(L + L_HV + hw * 128 + g * 16) & row0, h1 = *(const LAS u32x4*)(L + L_HV + hw * 128 + 64 + g * 16) & row0;
#pragma unroll
    for (int t = 0; t < 4; t++) {
      f32x4 a = {0.f, 0.f, 0.f, 0.f};
      a = mfma(h0, wb[t], a);
      y[t] = mfma(h1, wb[4 + t], a);
      PIN(y[t]);
    }
    wait_slot(1);
    const rsrc_t ro = mkrsrc(wo + (size_t)h * DH * DM, NOSIDE ? 0 : DH * DM * 2);
#pragma unroll
    for (int s = 0; s < 4; s++) load_step(ro, s, wb + 4 * s);
  }
  rsrc_t ry = mkrsrc(ws + WS_Y + (size_t)(b * 32 + 4 * hw) * 2048 + h * 64, 3 * 2048 + 64);
  u32 yoff = (c >> 2) * 2048 + (c & 3) * 16;
  asm volatile("" : "+s"(ry), "+v"(yoff), "+v"(tag));
  STAMP(st2);
  const LAS unsigned char* ap = L + L_OP + (c & 7) * OPS_ + g * 16;
  lds_barrier();
  {
    u32x4 a[4];
#pragma unroll
    for (int s = 0; s < 4; s++) a[s] = *(const LAS u32x4*)(ap + s * 64);
#pragma unroll
    for (int s = 0; s < 4; s++)
#pragma unroll
      for (int t = 0; t < 4; t++) y[t] = mfma(a[s], wb[4 * s + t], y[t]);
    f32x4 s;
#pragma unroll
    for (int t = 0; t < 4; t++) {
      const float u = (y[t][0] + y[t][1]) + (y[t][2] + y[t][3]);
      auto p = __builtin_amdgcn_permlane16_swap(asu(u), asu(u), false, false);
      s[t] = asf(p[0]) + asf(p[1]);
    }
    if (g == 0) __builtin_amdgcn_raw_buffer_store_b128(tagged<YM>(s, tag), ry, yoff, 0, YAUXS);
  }
  STAMP(st3);
  STAMP_OUT();
  __builtin_amdgcn_endpgm();
}

extern "C" void kernel_launch(void* const* d_in, const int* in_sizes, int n_in, void* d_out, int out_size, void* d_ws,
                              size_t ws_size, hipStream_t stream) {
  fused_decode<<<NB * NH, NW * 64, 0, stream>>>((const u16*)d_in[0], (const u16*)d_in[1], (const u16*)d_in[2], (const u16*)d_in[3],
                                                (const u16*)d_in[4], (const u16*)d_in[6], (const u16*)d_in[7], (const u16*)d_in[8],
                                                (const u16*)d_in[9], (u16*)d_out, (unsigned char*)d_ws);
}
```

```cpp
#include <hip/hip_runtime.h>
#include <cstdint>

typedef unsigned short u16;
typedef unsigned u32;
typedef unsigned long long u64;
typedef u32 u32x2 __attribute__((ext_vector_type(2)));
typedef u32 u32x4 __attribute__((ext_vector_type(4)));
typedef float f32x4 __attribute__((ext_vector_type(4)));
typedef short bf16x8 __attribute__((ext_vector_type(8)));
typedef __bf16 bf16x2 __attribute__((ext_vector_type(2)));
typedef __amdgpu_buffer_rsrc_t rsrc_t;
#define GAS __attribute__((address_space(1)))
#define LAS __attribute__((address_space(3)))
#define RLX_AGENT __ATOMIC_RELAXED, __HIP_MEMORY_SCOPE_AGENT

constexpr int WA = 16, WB = 20, WC = 22, WD = 26, WE = 30, LB = 40, LC = 80, LD = 130, ECL = 256, ELO = 0, EHI = 100;
constexpr int SCH_A = 35, SCH_T = 892, SCH_M = 33, DMAX = 40, DMIN = -10, DGAIN = 2;
constexpr int CKP = 256, CKEND = 200;
constexpr int P0 = 160, PSTEP = 20;
constexpr int JMIN = 160, JTYP = 200, JNEAR = 96, JMIN2 = 50;
constexpr int CSLEEP = 1, QSLEEP = 20, YFIRST = 28;

constexpr int NB = 8, DM = 512, NH = 32, SK = 4096, DH = 128, FF = 1792;
constexpr int NC = 4, NHOLD = 8, NW = 1 + NC + NHOLD + 1;
constexpr int MLPH = FF / 64;
constexpr int RING = 128;
constexpr int K0 = 16, V0 = K0 + 1024, END = V0 + 1024;
constexpr size_t WS_P = 4096, WS_Y = 65536, WS_Q = WS_Y + 8 * 32 * 2048;
constexpr u32 QM = 0x3fu, YM = 0xffu;
constexpr u32 SPIN_CAP = 1u << 20;
constexpr int AUX_SC1 = 16;
constexpr int OPS_ = DH * 2 + 16;
constexpr int L_SC = RING * 1024, L_HP = L_SC + SK * 4, L_OP = L_HP + 2 * NHOLD * 64 * 4, L_HV = L_OP + 2 * NC * OPS_, L_Q = L_HV + NHOLD * 128;
constexpr int L_FULL = L_Q + 256, L_NEXT = L_FULL + 256, L_QRDY = L_NEXT + 16, L_ML = L_QRDY + 16, L_MF = L_ML + 32, L_PF = L_MF + 16;
constexpr int L_HF = L_PF + 16, L_LATE = L_HF + 32, L_TAG = L_LATE + 8, L_END = L_NEXT + 256;
static_assert(L_TAG + 8 <= L_END && L_END <= 160 * 1024, "LDS map");
constexpr float SCALE = 0.08838834764831845f, L2E = 1.4426950408889634f;

__device__ __forceinline__ float asf(u32 v) { return __builtin_bit_cast(float, v); }
__device__ __forceinline__ u32 asu(float v) { return __builtin_bit_cast(u32, v); }
__device__ __forceinline__ float blo(u32 v) { return asf(v << 16); }
__device__ __forceinline__ float bhi(u32 v) { return asf(v & 0xffff0000u); }
__device__ __forceinline__ u32 cvtpk(float lo, float hi) {
  bf16x2 r = {(__bf16)lo, (__bf16)hi};
  return __builtin_bit_cast(u32, r);
}
__device__ __forceinline__ float rbf(float x) { return (float)(__bf16)x; }
__device__ __forceinline__ float dot2(u32 a, u32 b, float c) {
  return __builtin_amdgcn_fdot2_f32_bf16(__builtin_bit_cast(bf16x2, a), __builtin_bit_cast(bf16x2, b), c, false);
}
template <int CTRL> __device__ __forceinline__ float dpp(float x) {
  return asf(__builtin_amdgcn_mov_dpp(asu(x), CTRL, 0xf, 0xf, true));
}
constexpr int XOR1 = 0xB1, XOR2 = 0x4E, HMIR = 0x141, ROR8 = 0x128, ROR4 = 0x124;
__device__ __forceinline__ float xrow_max(float x) {
  auto s = __builtin_amdgcn_permlane16_swap(asu(x), asu(x), false, false);
  x = fmaxf(asf(s[0]), asf(s[1]));
  auto t = __builtin_amdgcn_permlane32_swap(asu(x), asu(x), false, false);
  return fmaxf(asf(t[0]), asf(t[1]));
}
__device__ __forceinline__ float xrow_sum(float x) {
  auto s = __builtin_amdgcn_permlane16_swap(asu(x), asu(x), false, false);
  x = asf(s[0]) + asf(s[1]);
  auto t = __builtin_amdgcn_permlane32_swap(asu(x), asu(x), false, false);
  return asf(t[0]) + asf(t[1]);
}
__device__ __forceinline__ float half_sum(float x) {
  x += dpp<XOR1>(x); x += dpp<XOR2>(x); x += dpp<HMIR>(x); return xrow_sum(x);
}
__device__ __forceinline__ float wave_sum(float x) { x = half_sum(x); return x + dpp<ROR8>(x); }
__device__ __forceinline__ float wave_max(float x) {
  x = fmaxf(x, dpp<XOR1>(x)); x = fmaxf(x, dpp<XOR2>(x)); x = fmaxf(x, dpp<HMIR>(x)); x = fmaxf(x, dpp<ROR8>(x)); return xrow_max(x);
}
__device__ __forceinline__ f32x4 mfma(u32x4 a, u32x4 b, f32x4 c) {
  return __builtin_amdgcn_mfma_f32_16x16x32_bf16(__builtin_bit_cast(bf16x8, a), __builtin_bit_cast(bf16x8, b), c, 0, 0, 0);
}
__device__ __forceinline__ rsrc_t mkrsrc(const void* p, u32 bytes) {
  return __builtin_amdgcn_make_buffer_rsrc((void*)p, 0, bytes, 0x00020000);
}
template <int AUX> __device__ __forceinline__ u32x4 bld(rsrc_t r, u32 voff, u32 soff) {
  return __builtin_amdgcn_raw_buffer_load_b128(r, voff, soff, AUX);
}
__device__ __forceinline__ u32 now32() { return (u32)__builtin_amdgcn_s_memrealtime(); }
#define rfl __builtin_amdgcn_readfirstlane
#define PIN(x) asm volatile("" : "+v"(x))
#define FENCE() asm volatile("" ::: "memory")
#define LGKM0() asm volatile("s_waitcnt lgkmcnt(0)" ::: "memory")
#define LDSW(off) (*(volatile LAS u32*)(L + (off)))
#define LDSW4(off) (*(volatile LAS u32x4*)(L + (off)))
#define SPIN(cond, nap) do { _Pragma("unroll 1") for (; budget; budget--) { if (cond) break; __builtin_amdgcn_s_sleep(nap); } FENCE(); } while (0)
__device__ __forceinline__ void lds_barrier() { asm volatile("s_waitcnt lgkmcnt(0)\n\ts_barrier" ::: "memory"); }
__device__ __forceinline__ u32 and4(u32x4 v) { return v[0] & v[1] & v[2] & v[3]; }
__device__ __forceinline__ u32 min4(u32x4 v) { return min(min(v[0], v[1]), min(v[2], v[3])); }
constexpr u32 TAG_STEP = 0x01010101u;
__device__ __forceinline__ u32x4 spread(u32 tag) { return u32x4{tag, tag >> 8, tag >> 16, tag >> 24}; }
template <u32 M> __device__ __forceinline__ u32x4 tagged(f32x4 v, u32x4 tg) {
  u32x4 r;
#pragma unroll
  for (int i = 0; i < 4; i++) r[i] = ((asu(v[i]) + (M + 1) / 2) & ~M) | (tg[i] & M);
  return r;
}
template <u32 M> __device__ __forceinline__ bool fresh(u32x4 a, u32x4 b, u32x4 tg) {
  const u32x4 d = (a ^ tg) | (b ^ tg);
  return ((d[0] | d[1] | d[2] | d[3]) & M) == 0u;
}
__device__ __forceinline__ u32 norm2(float xv, float mb, float den, float r) {
  const float a = rbf(xv - mb), q0 = a * r, qq = fmaf(fmaf(-q0, den, a), r, q0);
  return cvtpk(qq, qq);
}
__device__ __forceinline__ u32x4 pick_mask(int c) {
  u32x4 m;
#pragma unroll
  for (int r = 0; r < 4; r++) m[r] = (((c & 7) >> 1) == r) ? ((c & 1) ? 0xffff0000u : 0x0000ffffu) : 0u;
  return m;
}
__device__ __forceinline__ u32 sched(u32 n) { return (n * SCH_T) >> 8; }
__device__ __forceinline__ int stream_tile(rsrc_t r, u32 voff, u32 soff, u32 m0v, u32 fulladdr, u32 pos, int late, u32 lateaddr) {
  u32 cnt, lv;
#define QLD(off) "buffer_load_dwordx4 %[vo], %[r], %[so] offen offset:" #off " nt lds\n\t"
#define LOOP(lbl, w) #lbl ":\n\tv_subrev_u32 %[pub], %[" #w "]-4, %[pub]\n" \
    "0:\n\ts_waitcnt vmcnt(%[" #w "]-2)\n\t" QLD(0) QLD(1024) "ds_read_b32 %[lv], %[la]\n\ts_waitcnt vmcnt(%[" #w "]-2)\n\t" QLD(2048) QLD(3072) \
    "ds_max_i32 %[fa], %[pub]\n\ts_add_u32 m0, m0, 0x1000\n\ts_add_u32 %[so], %[so], 0x1000\n\ts_sub_u32 %[cnt], %[cnt], 1\n\t" \
    "v_add_u32 %[pub], 4, %[pub]\n\ts_cbranch_scc0 0b\n\t"
  asm volatile("s_mov_b32 m0, %[m0v]\n\ts_mov_b32 %[cnt], 3\n\t"
               "s_cmp_lt_i32 %[late], %[lb]\n\ts_cbranch_scc1 6f\n\t"
               "s_cmp_lt_i32 %[late], %[lc]\n\ts_cbranch_scc1 3f\n\t"
               "s_cmp_lt_i32 %[late], %[ld]\n\ts_cbranch_scc1 4f\n\t"
               LOOP(5, we) "s_branch 9f\n" LOOP(4, wd) "s_branch 9f\n" LOOP(3, wc) "s_branch 9f\n"
               "6:\n\ts_cmp_lt_i32 %[late], 0\n\ts_cbranch_scc1 1f\n\t"
               LOOP(2, wb) "s_branch 9f\n" LOOP(1, wa) "\n9:\n\ts_waitcnt lgkmcnt(0)"
               : [cnt] "=&s"(cnt), [so] "+&s"(soff), [pub] "+&v"(pos), [lv] "=&v"(lv)
               : [vo] "v"(voff), [r] "s"(r), [m0v] "s"(m0v), [fa] "v"(fulladdr), [la] "v"(lateaddr), [late] "s"(late), [lb] "i"(LB), [lc] "i"(LC), [ld] "i"(LD),
                 [wa] "i"(WA), [wb] "i"(WB), [wc] "i"(WC), [wd] "i"(WD), [we] "i"(WE)
               : "memory", "scc");
  return rfl(lv);
}

__global__ __launch_bounds__(NW * 64, 4) void fused_decode(
    const u16* __restrict__ Kc, const u16* __restrict__ wq, const u16* __restrict__ x, unsigned char* __restrict__ ws,
    const u16* __restrict__ Vc, const u16* __restrict__ b2, u16* __restrict__ out, const u16* __restrict__ wo,
    const u16* __restrict__ w1, const u16* __restrict__ b1, const u16* __restrict__ w2) {
  __shared__ __attribute__((aligned(16))) unsigned char lds_raw[L_END];
  LAS unsigned char* L = (LAS unsigned char*)lds_raw;
  const u32 tstart = now32();
  const int tid = threadIdx.x, lane = tid & 63, w = rfl(tid >> 6);
  const int g = lane >> 4, c = lane & 15;
  const int bid = blockIdx.x, h = (bid & 7) * 4 + ((bid >> 3) & 3), b = bid >> 5;
  const u32 l16 = lane * 16;
  u32 budget = SPIN_CAP;
  if (w == 0) *(volatile LAS u32x2*)(L + L_FULL + lane * 8) = u32x2{0u, 0u};
  asm volatile("" :: "s"(Kc), "s"(wq), "s"(x), "s"(ws));
  lds_barrier();

  if (w > NC && w < NW - 1) {
    const int hw = w - 1 - NC;
    const bool has_mlp = h < MLPH;
    const int hm = has_mlp ? h : 0;
    f32x4 y[4];
    u32x4 wb[16];
    const u32 off2 = g * (8 * DM * 2) + c * 8;
    auto load_step = [&](rsrc_t r, int s, u32x4* dst) {
      u32x2 raw[8];
#pragma unroll
      for (int j = 0; j < 8; j++) raw[j] = __builtin_amdgcn_raw_buffer_load_b64(r, off2 + (j & 3) * (DM * 2), (32 * s + (j & 4)) * (DM * 2) + hw * 128, 0);
#pragma unroll
      for (int t = 0; t < 4; t++) {
#pragma unroll
        for (int j = 0; j < 4; j++) dst[t][j] = __builtin_amdgcn_perm(raw[2 * j + 1][t >> 1], raw[2 * j][t >> 1], (t & 1) ? 0x07060302u : 0x05040100u);
        PIN(dst[t]);
      }
    };
    auto wait_slot = [&](int step) {
      const u32 due = P0 + (step * 8 + ((hw + b) & 7) + (h & 7)) * PSTEP;
      SPIN(rfl(LDSW(L_FULL)) >= due, 6);
    };
    wait_slot(0);
    const rsrc_t rx = mkrsrc(x + b * DM, DM * 2);
    const rsrc_t r1 = mkrsrc(w1 + hm * 64, has_mlp ? (DM * FF - hm * 64) * 2 : 0);
    const rsrc_t r2 = mkrsrc(w2 + (size_t)hm * 64 * DM, has_mlp ? 64 * DM * 2 : 0);
    const u32x4 x8 = bld<0>(rx, l16, 0), xa = bld<0>(rx, (lane >> 3) * 16, hw * 128);
    const float b1v = asf((u32)b1[64 * hm + lane] << 16);
    const u32 off1 = (lane >> 3) * (8 * FF * 2) + (lane & 7) * 16;
#pragma unroll
    for (int i = 0; i < 8; i++) { if (i == 4) wait_slot(1); wb[8 + i] = bld<0>(r1, off1, (64 * hw + i) * (FF * 2)); }
    {
      float s = 0.f;
#pragma unroll
      for (int j = 0; j < 4; j++) s += blo(x8[j]) + bhi(x8[j]);
      const float mean = wave_sum(s) * (1.f / DM);
      float q = 0.f;
#pragma unroll
      for (int j = 0; j < 4; j++) { const float d0 = blo(x8[j]) - mean, d1 = bhi(x8[j]) - mean; q = fmaf(d0, d0, q); q = fmaf(d1, d1, q); }
      q = wave_sum(q);
      const float mb = rbf(mean), den = rbf(__builtin_amdgcn_sqrtf(rbf(rbf(q * (1.f / DM)) + 1e-5f))), r = __builtin_amdgcn_rcpf(den);
      const u32x4 m8 = pick_mask(c);
      f32x4 acc = {0.f, 0.f, 0.f, 0.f};
#pragma unroll
      for (int i = 0; i < 8; i++) acc = mfma(m8 & norm2((i & 1) ? bhi(xa[i >> 1]) : blo(xa[i >> 1]), mb, den, r), wb[8 + i], acc);
      if ((g >> 1) == (c >> 3)) *(LAS f32x4*)(L + L_HP + ((2 * hw + (g >> 1)) * 64 + 8 * (c & 7) + 4 * (g & 1)) * 4) = acc;
      LGKM0();
      LDSW(L_HF + hw * 4) = 1u;
    }
    wait_slot(2);
    load_step(r2, 0, wb);
    wait_slot(3);
    load_step(r2, 1, wb + 4);
    SPIN(rfl(and4(LDSW4(L_HF)) & and4(LDSW4(L_HF + 16))), 16);
    {
      float hs = 0.f;
#pragma unroll
      for (int i = 0; i < 2 * NHOLD; i++) hs += *(const LAS float*)(L + L_HP + (i * 64 + lane) * 4);
      const float hv = rbf(rbf(hs) + b1v);
      const float e = __builtin_amdgcn_exp2f((2.f * L2E * 0.7978845608028654f) * (hv + 0.044715f * hv * hv * hv));
      const float th = 1.f - 2.f / (e + 1.f);
      float gl = 0.5f * hv * (1.f + th);
      gl = has_mlp ? gl : 0.f;
      *(LAS u16*)(L + L_HV + hw * 128 + lane * 2) = (u16)cvtpk(gl, gl);
      LGKM0();
      const u32 row0 = c == 0 ? ~0u : 0u;
      const u32x4 h0 = *(const LAS u32x4*)(L + L_HV + hw * 128 + g * 16) & row0, h1 = *(const LAS u32x4*)(L + L_HV + hw * 128 + 64 + g * 16) & row0;
#pragma unroll
      for (int t = 0; t < 4; t++) {
        f32x4 a = {0.f, 0.f, 0.f, 0.f};
        a = mfma(h0, wb[t], a);
        y[t] = mfma(h1, wb[4 + t], a);
        PIN(y[t]);
      }
    }
    const rsrc_t ro = mkrsrc(wo + (size_t)h * DH * DM, DH * DM * 2);
#pragma unroll
    for (int s = 0; s < 4; s++) { wait_slot(4 + s); load_step(ro, s, wb + 4 * s); }
    SPIN(rfl(LDSW(L_TAG + 4)), 4);
    u32x4 tg = spread(LDSW(L_TAG));
    rsrc_t ry = mkrsrc(ws + WS_Y + (size_t)(b * 32 + 4 * hw) * 2048 + h * 64, 3 * 2048 + 64);
    u32 yoff = (c >> 2) * 2048 + (c & 3) * 16;
    asm volatile("" : "+s"(ry));
    PIN(yoff); PIN(tg);
    const LAS unsigned char* ap = L + L_OP + (c & 7) * OPS_ + g * 16;
    lds_barrier();
    u32x4 a[4];
#pragma unroll
    for (int s = 0; s < 4; s++) a[s] = *(const LAS u32x4*)(ap + s * 64);
    __builtin_amdgcn_sched_barrier(0);
#pragma unroll
    for (int s = 0; s < 4; s++)
#pragma unroll
      for (int t = 0; t < 4; t++) y[t] = mfma(a[s], wb[4 * s + t], y[t]);
    __builtin_amdgcn_sched_barrier(0);
    f32x4 s;
#pragma unroll
    for (int t = 0; t < 4; t++) {
      const float u = (y[t][0] + y[t][1]) + (y[t][2] + y[t][3]);
      auto p = __builtin_amdgcn_permlane16_swap(asu(u), asu(u), false, false);
      s[t] = asf(p[0]) + asf(p[1]);
    }
    if (g == 0) __builtin_amdgcn_raw_buffer_store_b128(tagged<YM>(s, tg), ry, yoff, 0, AUX_SC1);
    __builtin_amdgcn_endpgm();
  }

  if (w == NW - 1) {
    GAS u32* ep = (GAS u32*)ws;
    u32 tag = __hip_atomic_load(ep, RLX_AGENT);
    u32x2 xv = *(const u32x2*)(x + b * DM + 16 * h + 4 * (lane & 3)), bv = *(const u32x2*)(b2 + 16 * h + 4 * (lane & 3));
    tag += TAG_STEP;
    const u32x4 tg = spread(tag);
    *(volatile LAS u32x2*)(L + L_TAG) = u32x2{tag, 1u};
    const rsrc_t rp = mkrsrc(ws + WS_P, (END / CKP + 1) * 512);
    if (lane == bid) __builtin_amdgcn_raw_buffer_store_b64(u32x2{tstart, tag}, rp, lane * 8, 0, AUX_SC1);
    u32x4 g0, g1;
    {
      const rsrc_t rqs = mkrsrc(ws + WS_Q + (size_t)(h * 8 + b) * 2048, 2048);
      SPIN(rfl(min4(LDSW4(L_NEXT))), 2);
#pragma unroll 1
      for (;; budget--) {
        __builtin_amdgcn_s_sleep(QSLEEP);
        FENCE();
        g0 = bld<AUX_SC1>(rqs, l16, 0); g1 = bld<AUX_SC1>(rqs, l16 + 1024, 0);
        if (__all(fresh<QM>(g0, g1, tg)) || !budget) break;
      }
      float qa[4], qb[4];
#pragma unroll
      for (int i = 0; i < 4; i++) { qa[i] = xrow_sum(asf(g0[i] & ~QM)); qb[i] = xrow_sum(asf(g1[i] & ~QM)); }
      if (g == 0) *(LAS u32x2*)(L + L_Q + c * 8) = u32x2{cvtpk(qa[0], qa[1]), cvtpk(qa[2], qa[3])};
      if (g == 1) *(LAS u32x2*)(L + L_Q + 128 + c * 8) = u32x2{cvtpk(qb[0], qb[1]), cvtpk(qb[2], qb[3])};
      LGKM0();
      LDSW(L_QRDY) = 1u;
    }
    {
      u32 t0 = tstart + SCH_A, k = 1, pf = rfl(LDSW(L_LATE + 4)), praw = now32() - sched(pf), jump = 0, jpos = 0, p0 = 0, pmean = 0;
      int drift = 0;
#pragma unroll 1
      for (; budget; budget--) {
        const u32 f = rfl(LDSW(L_LATE + 4));
        if (f >= END - 16) break;
        if (f == pf) { __builtin_amdgcn_s_sleep(CSLEEP); continue; }
        const u32 now = now32(), raw = now - sched(f);
        const int d = (int)(raw - praw);
        if (d > JMIN) { jump += JTYP; jpos = f; }
        else if (jump && f - jpos <= JNEAR && d > JMIN2) { jump += d; jpos = f; }
        pf = f; praw = raw;
        int late = (int)(raw - jump - t0) - drift * (int)(f - p0) / CKP;
        if (f < ECL) late = min(max(late, ELO), EHI);
        LDSW(L_LATE) = late;
        if (f >= k * CKP && k * CKP < END - CKEND) {
          if (lane == bid) __builtin_amdgcn_raw_buffer_store_b64(u32x2{raw, tag}, rp, k * 512 + lane * 8, 0, AUX_SC1);
          const u32x2 v = __builtin_amdgcn_raw_buffer_load_b64(rp, (k - 1) * 512 + lane * 8, 0, AUX_SC1);
          const bool ok = v[1] == tag;
          const float cnt = (float)__builtin_popcountll(__ballot(ok));
          const float sum = wave_sum(ok ? (float)(int)(v[0] - raw) : 0.f);
          if (cnt >= 32.f) {
            const u32 mean = raw + rfl((int)(sum * __builtin_amdgcn_rcpf(cnt)));
            t0 = k > 1 ? mean - SCH_M : mean + SCH_A;
            p0 = (k - 1) * CKP;
            if (k > 2 && (int)(mean - pmean) < DMAX) drift = max((int)(mean - pmean), DMIN) * DGAIN / 4;
            pmean = mean;
            if (p0 >= jpos) jump = 0;
          }
          k++;
          pf = rfl(LDSW(L_LATE + 4));
        }
      }
    }
    f32x4 base = {blo(xv[0]) + blo(bv[0]), bhi(xv[0]) + bhi(bv[0]), blo(xv[1]) + blo(bv[1]), bhi(xv[1]) + bhi(bv[1])};
    PIN(base);
    const rsrc_t rs = mkrsrc(ws + WS_Y + (size_t)(b * 32 + h) * 2048, 2048);
    lds_barrier();
    __builtin_amdgcn_s_sleep(YFIRST);
#pragma unroll 1
    for (;; budget--) {
      FENCE();
      g0 = bld<AUX_SC1>(rs, l16, 0); g1 = bld<AUX_SC1>(rs, l16 + 1024, 0);
      if (__all(fresh<YM>(g0, g1, tg)) || !budget) break;
    }
    float s[4];
#pragma unroll
    for (int i = 0; i < 4; i++) {
      float u = asf(g0[i] & ~YM) + asf(g1[i] & ~YM);
      u += dpp<ROR4>(u); u += dpp<ROR8>(u);
      s[i] = xrow_sum(u) + base[i];
    }
    if (lane < 4) *(u32x2*)(out + b * DM + 16 * h + 4 * lane) = u32x2{cvtpk(s[0], s[1]), cvtpk(s[2], s[3])};
    if (bid == 0 && lane == 0) __hip_atomic_store(ep, tag, RLX_AGENT);
    __builtin_amdgcn_endpgm();
  }

  if ((u32)(w - 1) < (u32)NC) {
    const int cw = w - 1;
    const u32x4 m8 = pick_mask(c);
    u32x4 t[16];
    {
      const rsrc_t rx = mkrsrc(x + (2 * cw) * DM, 2 * DM * 2);
      const u32 xb = (c >> 3) * (DM * 2);
      const u32x4 s0 = bld<0>(rx, xb + (g * 8 + (c & 7)) * 32, 0), s1 = bld<0>(rx, xb + (g * 8 + (c & 7)) * 32 + 16, 0);
      const u32x2 xq = __builtin_amdgcn_raw_buffer_load_b64(rx, xb + (g * 8 + (c & 7)) * 8, (b >> 1) * 256, 0);
      float s = 0.f;
#pragma unroll
      for (int j = 0; j < 4; j++) s += (blo(s0[j]) + bhi(s0[j])) + (blo(s1[j]) + bhi(s1[j]));
      const float mean = half_sum(s) * (1.f / DM);
      float q = 0.f;
#pragma unroll
      for (int j = 0; j < 4; j++) {
        const float d0 = blo(s0[j]) - mean, d1 = bhi(s0[j]) - mean, d2 = blo(s1[j]) - mean, d3 = bhi(s1[j]) - mean;
        q = fmaf(d0, d0, q); q = fmaf(d1, d1, q); q = fmaf(d2, d2, q); q = fmaf(d3, d3, q);
      }
      q = half_sum(q);
      const float mb = rbf(mean), den = rbf(__builtin_amdgcn_sqrtf(rbf(rbf(q * (1.f / DM)) + 1e-5f))), r = __builtin_amdgcn_rcpf(den);
      LAS unsigned char* xl = L + L_SC + cw * 1024 + (c >> 3) * 512;
      *(LAS u32x4*)(xl + (g * 8 + (c & 7)) * 16) =
          u32x4{norm2(blo(xq[0]), mb, den, r), norm2(bhi(xq[0]), mb, den, r), norm2(blo(xq[1]), mb, den, r), norm2(bhi(xq[1]), mb, den, r)};
      LGKM0();
      SPIN(rfl(LDSW(L_FULL)) >= (u32)K0, 1);
      f32x4 a0 = {0.f, 0.f, 0.f, 0.f}, a1 = a0;
#pragma unroll 1
      for (int i = 0; i < 4; i++) {
        const u32x4 e0 = *(const LAS u32x4*)(xl + g * 128 + i * 16), e1 = *(const LAS u32x4*)(xl + g * 128 + 64 + i * 16);
#pragma unroll
        for (int k = 0; k < 4; k++) t[k] = *(const LAS u32x4*)(L + (4 * i + k) * 1024 + l16);
#pragma unroll
        for (int k = 0; k < 4; k++) { a0 = mfma(m8 & e0[k], t[k], a0); a1 = mfma(m8 & e1[k], t[k], a1); }
      }
      LGKM0();
      LDSW(L_NEXT + cw * 4) = K0 + 16 * cw;
      f32x4 z;
#pragma unroll
      for (int i = 0; i < 4; i++) { const float u = c < 8 ? a0[i] : a1[i]; z[i] = u + dpp<ROR8>(u); }
      SPIN(rfl(LDSW(L_TAG + 4)), 1);
      const u32x4 tg = spread(LDSW(L_TAG));
      const rsrc_t rqs = mkrsrc(ws + WS_Q + (size_t)(h * 8 + 2 * cw) * 2048 + ((b & 1) * 4 + (b >> 1)) * 256, 2048 + 256);
      if (c < 8) __builtin_amdgcn_raw_buffer_store_b128(tagged<QM>(z, tg), rqs, (g >> 1) * 2048 + (2 * c + (g & 1)) * 16, 0, AUX_SC1);
    }
    u32x4 qp;
    float mx = -INFINITY;
#pragma unroll 1
    for (int i = 0; i < 16; i++) {
      const u32 n = K0 + 16 * (cw + NC * i);
      SPIN(rfl(LDSW(L_FULL)) >= n + 16, 2);
      const LAS unsigned char* src = L + (n & (RING - 1)) * 1024 + l16;
#pragma unroll
      for (int k = 0; k < 16; k++) t[k] = *(const LAS u32x4*)(src + k * 1024);
      LGKM0();
      LDSW(L_NEXT + cw * 4) = i < 15 ? n + 16 * NC : V0 + 4 * cw;
      if (i == 0) {
        SPIN(rfl(LDSW(L_QRDY)), 1);
        qp = *(const LAS u32x4*)(L + L_Q + c * 16);
      }
      float s[16];
#pragma unroll
      for (int k = 0; k < 16; k++) {
        PIN(t[k]);
        float d = dot2(t[k][0], qp[0], 0.f);
        d = dot2(t[k][1], qp[1], d); d = dot2(t[k][2], qp[2], d); s[k] = dot2(t[k][3], qp[3], d);
      }
#define TR_STEP(HALF, CTRL, BIT) _Pragma("unroll") for (int k = 0; k < (HALF); k++) { \
        const float lo_ = s[k], hi_ = s[k + (HALF)]; const bool up_ = (c & (BIT)) != 0; \
        s[k] = (up_ ? hi_ : lo_) + dpp<CTRL>(up_ ? lo_ : hi_); }
      TR_STEP(8, ROR8, 8) TR_STEP(4, HMIR, 4) TR_STEP(2, XOR2, 2) TR_STEP(1, XOR1, 1)
#undef TR_STEP
      const float sc = rbf(rbf(s[0]) * SCALE);
      mx = fmaxf(mx, sc);
      *(LAS float*)(L + L_SC + ((cw + NC * i) * 64 + lane) * 4) = sc;
    }
    {
      mx = wave_max(mx);
      float e[16], ls = 0.f;
#pragma unroll
      for (int i = 0; i < 16; i++) {
        e[i] = __builtin_amdgcn_exp2f((*(const LAS float*)(L + L_SC + ((cw + NC * i) * 64 + lane) * 4) - mx) * L2E);
        ls += e[i];
      }
      ls = wave_sum(ls);
      LDSW(L_ML + cw * 4) = asu(mx); LDSW(L_ML + 16 + cw * 4) = asu(ls);
      LGKM0();
      LDSW(L_MF + cw * 4) = 1u;
      SPIN(rfl(and4(LDSW4(L_MF))), 1);
      const f32x4 ma = *(volatile LAS f32x4*)(L + L_ML), la = *(volatile LAS f32x4*)(L + L_ML + 16);
      const float M = fmaxf(fmaxf(ma[0], ma[1]), fmaxf(ma[2], ma[3]));
      float Ls = 0.f;
#pragma unroll
      for (int i = 0; i < 4; i++) Ls = fmaf(__builtin_amdgcn_exp2f((ma[i] - M) * L2E), la[i], Ls);
      const float f = __builtin_amdgcn_exp2f((mx - M) * L2E) / Ls;
#pragma unroll
      for (int i = 0; i < 16; i++) { const float p = e[i] * f; *(LAS u32*)(L + L_SC + ((cw + NC * i) * 64 + lane) * 4) = cvtpk(p, p); }
      LGKM0();
      LDSW(L_PF + cw * 4) = 1u;
      SPIN(rfl(and4(LDSW4(L_PF))), 1);
    }
    const u32x4 m4 = m8 & (c < 8 ? ~0u : 0u);
    f32x4 oacc = {0.f, 0.f, 0.f, 0.f};
#pragma unroll 1
    for (int i = 0; i < 64; i++) {
      const u32 n = V0 + 16 * i + 4 * cw;
      const u32x4 pp = *(const LAS u32x4*)(L + L_SC + (i * 64 + g * 16 + 4 * cw) * 4);
      u32x4 pa[4];
#pragma unroll
      for (int k = 0; k < 4; k++) { pa[k] = m4 & pp[k]; PIN(pa[k]); }
      const LAS unsigned char* src = L + (n & (RING - 1)) * 1024 + l16;
      SPIN(rfl(LDSW(L_FULL)) >= n + 4, 1);
#pragma unroll
      for (int k = 0; k < 4; k++) t[k] = *(const LAS u32x4*)(src + k * 1024);
#pragma unroll
      for (int k = 0; k < 4; k++) oacc = mfma(pa[k], t[k], oacc);
      PIN(oacc);
      LGKM0();
      LDSW(L_NEXT + cw * 4) = n + 16;
    }
    {
      const u32x2 hi = {cvtpk(oacc[0], oacc[1]), cvtpk(oacc[2], oacc[3])};
      const u32x2 lo = {cvtpk(oacc[0] - blo(hi[0]), oacc[1] - bhi(hi[0])), cvtpk(oacc[2] - blo(hi[1]), oacc[3] - bhi(hi[1]))};
      LAS unsigned char* op = L + L_OP + 2 * cw * OPS_ + (8 * c + 4 * g) * 2;
      if (lane < 32) { *(LAS u32x2*)op = hi; *(LAS u32x2*)(op + OPS_) = lo; }
    }
    lds_barrier();
    __builtin_amdgcn_endpgm();
  }

  __builtin_amdgcn_s_setprio(3);
  const u32 ring0 = (u32)(uintptr_t)L;
  {
    const int qc = h * DH + 64 * (b & 1);
    const rsrc_t rq = mkrsrc(wq + (size_t)(128 * (b >> 1)) * (NH * DH) + qc, (128 * NH * DH - qc) * 2);
    const u32 qoff = (lane >> 3) * (16 * NH * DH * 2) + (lane & 7) * 16;
    u32 so;
    asm volatile("s_mov_b32 m0, %[m0v]\n\ts_mov_b32 %[so], 0\n"
                 "0:\n\tbuffer_load_dwordx4 %[vo], %[r], %[so] offen nt lds\n\ts_add_u32 m0, m0, 0x400\n\ts_add_u32 %[so], %[so], %[st]\n\t"
                 "s_cmp_lt_u32 %[so], %[en]\n\ts_cbranch_scc1 0b"
                 : [so] "=&s"(so) : [vo] "v"(qoff), [r] "s"(rq), [m0v] "s"(ring0), [st] "i"(NH * DH * 2), [en] "i"(16 * NH * DH * 2) : "memory", "scc");
  }
  const size_t bh = (size_t)(b * NH + h) * (SK * DH);
  const u32 fulladdr = ring0 + L_FULL + lane * 4;
  u32 limit = RING;
  int late = LD;
#pragma unroll 1
  for (u32 n = K0; n < END; n += 16) {
    const rsrc_t r = mkrsrc((n < V0 ? Kc : Vc) + bh, SK * DH * 2);
    if (__builtin_expect(n + 16 > limit, 0)) {
#pragma unroll 1
      for (u32 spins = 0; budget; spins++, budget--) {
        limit = rfl(min4(LDSW4(L_NEXT))) + RING;
        if (n + 16 <= limit) break;
        if (spins == 0) { asm volatile("s_waitcnt vmcnt(0)" ::: "memory"); LDSW(L_FULL + lane * 4) = n; }
        __builtin_amdgcn_s_sleep(1);
      }
    }
    LDSW(L_LATE + 4) = n;
    late = stream_tile(r, l16, ((n - K0) & 1023) * 1024, ring0 + (n & (RING - 1)) * 1024, fulladdr, n, late, ring0 + L_LATE);
  }
#pragma unroll
  for (int k = (WE - 2) / 4 * 4; k >= 0; k -= 4)
    asm volatile("s_waitcnt vmcnt(%2)\n\tds_max_i32 %0, %1" :: "v"(fulladdr), "v"(END - k), "i"(k) : "memory");
  LGKM0();
  __builtin_amdgcn_endpgm();
}

extern "C" void kernel_launch(void* const* d_in, const int* in_sizes, int n_in, void* d_out, int out_size, void* d_ws,
                              size_t ws_size, hipStream_t stream) {
  fused_decode<<<NB * NH, NW * 64, 0, stream>>>((const u16*)d_in[1], (const u16*)d_in[3], (const u16*)d_in[0], (unsigned char*)d_ws,
                                                (const u16*)d_in[2], (const u16*)d_in[9], (u16*)d_out, (const u16*)d_in[4],
                                                (const u16*)d_in[6], (const u16*)d_in[7], (const u16*)d_in[8]);
}
```
